```python
import torch

def _softmax(x, axis=-1):
    return torch.softmax(x, dim=axis)
def _gelu(x):
    return torch.nn.functional.gelu(x, approximate="tanh")
def _silu(x):
    return torch.nn.functional.silu(x)
def _sigmoid(x):
    return torch.sigmoid(x)
def _layernorm(x, axis=-1, eps=1e-5):
    m = x.mean(dim=axis, keepdim=True)
    v = x.var(dim=axis, keepdim=True, unbiased=False)
    return (x - m) / torch.sqrt(v + eps)
def _scatter_add(tgt, idx, src, axis=0):
    return tgt.clone().index_add_(axis, idx, src)
def _segment_sum(src, seg, n):
    out = torch.zeros((n,) + src.shape[1:], dtype=src.dtype)
    return out.index_add_(0, seg, src)
def _pad(x, pw):
    flat = tuple(v for pair in reversed(pw) for v in pair)
    return torch.nn.functional.pad(x, flat)

kernel_name = 'parallel_sum__mha_block__glu_gated_bilinear__prompt'

def setup_inputs():

    _w = (lambda a: (lambda t: t.to(torch.bfloat16) if t.is_floating_point() else t)(torch.as_tensor(a)))
    def _A_setup():
        B, S, D, H = 1, 2048, 640, 32
        return {
            "x": _w((lambda *s: torch.randn(*s, dtype=torch.bfloat16) * 0.05)(B, S, D)),
            "wqkv": _w((lambda *s: torch.randn(*s, dtype=torch.bfloat16) * 0.05)(D, 3 * D)),
            "wo": _w((lambda *s: torch.randn(*s, dtype=torch.bfloat16) * 0.05)(D, D)),
        }
    _A_in = _A_setup()

    def _B_setup():
        B, S, D, F = 1, 2048, 640, 1536
        return {
            "x": _w((lambda *s: torch.randn(*s, dtype=torch.bfloat16) * 0.05)(B, S, D)),
            "w_gate": _w((lambda *s: torch.randn(*s, dtype=torch.bfloat16) * 0.8)(D, F) * (D ** -0.5)),
            "w_up": _w((lambda *s: torch.randn(*s, dtype=torch.bfloat16) * 0.8)(D, F) * (D ** -0.5)),
            "w_down": _w((lambda *s: torch.randn(*s, dtype=torch.bfloat16) * 0.8)(F, D) * (F ** -0.5)),
        }
    _B_in = _B_setup()

    return {
        "A_x": _A_in["x"],
        "A_wqkv": _A_in["wqkv"],
        "A_wo": _A_in["wo"],
        "B_x": _B_in["x"],
        "B_w_gate": _B_in["w_gate"],
        "B_w_up": _B_in["w_up"],
        "B_w_down": _B_in["w_down"],
    }

def reference(A_x, A_wqkv, A_wo, B_x, B_w_gate, B_w_up, B_w_down):
    def _A_ref(x, wqkv, wo):
        B, S, D = x.shape
        H = 32
        Dh = D // H
        qkv = (lambda x, *d: x.permute(*d))((x @ wqkv).reshape(B, S, 3, H, Dh), 2, 0, 3, 1, 4)
        q, k, v = qkv[0], qkv[1], qkv[2]
        scores = torch.einsum("bhqd,bhkd->bhqk", q, k) * (Dh ** -0.5)
        p = _softmax(scores, axis=-1)
        o = torch.einsum("bhqk,bhkd->bhqd", p, v)
        return (lambda x, *d: x.permute(*d))(o, 0, 2, 1, 3).reshape(B, S, D) @ wo

    def _B_ref(x, w_gate, w_up, w_down):
        g = torch.einsum("bsd,df->bsf", x, w_gate)
        u = torch.einsum("bsd,df->bsf", x, w_up)
        h = g * u
        return torch.einsum("bsf,fd->bsd", h, w_down)

    x = A_x
    xn = _layernorm(x, axis=-1)
    return x + _A_ref(xn, A_wqkv, A_wo) + _B_ref(xn, B_w_gate, B_w_up, B_w_down)
```

```cpp
#include <hip/hip_runtime.h>
#include <hip/hip_bf16.h>

#define FUSED 1
#define STAMPS 0

typedef __bf16 bf16x8 __attribute__((ext_vector_type(8)));
typedef __bf16 bf16x2 __attribute__((ext_vector_type(2)));
typedef short s16x4 __attribute__((ext_vector_type(4)));
typedef float f32x16 __attribute__((ext_vector_type(16)));
typedef float f32x4 __attribute__((ext_vector_type(4)));
typedef unsigned u32x4 __attribute__((ext_vector_type(4)));
typedef unsigned u32x2 __attribute__((ext_vector_type(2)));
typedef unsigned long long u64;
#define LAS __attribute__((address_space(3)))
#define GAS __attribute__((address_space(1)))
#define DEV __device__ __forceinline__

constexpr unsigned WS_EPOCH = 0;
constexpr unsigned WS_FA = 4096;
constexpr unsigned WS_FH = 8192;
constexpr unsigned WS_FO = 16384;
constexpr unsigned WS_Q = 65536;
constexpr unsigned WS_KV = WS_Q + 16 * 64 * 2560;
constexpr unsigned WS_HP = WS_KV + 16 * 64 * 5120;
constexpr unsigned WS_WP = WS_HP + 128 * 272 * 256;
constexpr unsigned WS_END = WS_WP + 40 * 68 * 1024;
constexpr unsigned WS_STAMP = 32u << 20;
constexpr unsigned OOB = 0x7fffff00u;

constexpr int L_ROWP = 131072;
constexpr int L_COLP = L_ROWP + 2048;
constexpr int L_PRIV = L_COLP + 3072;
constexpr int L_FLAG = L_PRIV + 4096;
constexpr int L_TOTAL = 163840;

constexpr float QSCALE = 0.22360679774997896f * 1.4426950408889634f;
constexpr int SC1 = 16;

DEV unsigned pk2(float lo, float hi) { bf16x2 v = {(__bf16)lo, (__bf16)hi}; return __builtin_bit_cast(unsigned, v); }
DEV float bfr(float x) { return (float)(__bf16)x; }
DEV float swap_add(float v) {
    auto r = __builtin_amdgcn_permlane32_swap(__float_as_uint(v), __float_as_uint(v), false, false);
    return __uint_as_float(r[0]) + __uint_as_float(r[1]);
}
DEV float swap_max(float v) {
    auto r = __builtin_amdgcn_permlane32_swap(__float_as_uint(v), __float_as_uint(v), false, false);
    return fmaxf(__uint_as_float(r[0]), __uint_as_float(r[1]));
}
DEV void glds16(unsigned voff, const void* sbase, unsigned ldsdst) {
    asm volatile("s_mov_b32 m0, %2\n\ts_nop 0\n\tglobal_load_lds_dwordx4 %0, %1" ::"v"(voff), "s"(sbase), "s"(ldsdst) : "memory");
}
template <int N> DEV void wait_bar() { asm volatile("s_waitcnt vmcnt(%0) lgkmcnt(0)\n\ts_barrier" ::"i"(N) : "memory"); }
DEV void lds_bar() { asm volatile("s_waitcnt lgkmcnt(0)\n\ts_barrier" ::: "memory"); }
DEV bf16x8 lds128(const LAS char* p) { return *(const LAS bf16x8*)p; }
DEV bf16x8 ldstr(const LAS char* p0, const LAS char* p1) {
    s16x4 a = __builtin_amdgcn_ds_read_tr16_b64_v4i16((LAS s16x4*)p0), b = __builtin_amdgcn_ds_read_tr16_b64_v4i16((LAS s16x4*)p1);
    return __builtin_bit_cast(bf16x8, __builtin_shufflevector(a, b, 0, 1, 2, 3, 4, 5, 6, 7));
}
DEV float dot_ones(bf16x8 v, float acc) {
    const bf16x2 one = {(__bf16)1.0f, (__bf16)1.0f};
#pragma unroll
    for (int i = 0; i < 4; ++i) acc = __builtin_amdgcn_fdot2_f32_bf16((bf16x2){v[2 * i], v[2 * i + 1]}, one, acc, false);
    return acc;
}
DEV float dot_self(bf16x8 v, float acc) {
#pragma unroll
    for (int i = 0; i < 4; ++i) { bf16x2 p = {v[2 * i], v[2 * i + 1]}; acc = __builtin_amdgcn_fdot2_f32_bf16(p, p, acc, false); }
    return acc;
}
#define MFMA32(a, b, c) __builtin_amdgcn_mfma_f32_32x32x16_bf16(a, b, c, 0, 0, 0)
#define MFMA16(a, b, c) __builtin_amdgcn_mfma_f32_16x16x32_bf16(a, b, c, 0, 0, 0)

#define STAMP(i) do {} while (0)
#define STAMP_DECL
#define STAMP_OUT do {} while (0)

template <int NPER> DEV void poll_flags(const char* ws, unsigned off, unsigned stride_k, bool active, u64 tag) {
    for (unsigned spin = 0; spin < (1u << 22); ++spin) {
        bool ok = true;
#pragma unroll
        for (int k = 0; k < NPER; ++k) {
            u64 v = __hip_atomic_load((const GAS u64*)(ws + off + k * stride_k), __ATOMIC_RELAXED, __HIP_MEMORY_SCOPE_AGENT);
            ok &= (v == tag) || !active;
        }
        if (__all(ok)) break;
        __builtin_amdgcn_s_sleep(1);
    }
    __builtin_amdgcn_fence(__ATOMIC_ACQUIRE, "wavefront");
}

#define EXP4(a, b, c, d) "v_exp_f32 v" #a ", v" #a "\n\tv_exp_f32 v" #b ", v" #b "\n\tv_exp_f32 v" #c ", v" #c "\n\tv_exp_f32 v" #d ", v" #d "\n\t"
#define E_S0_0 EXP4(64, 65, 66, 67)
#define E_S0_1 EXP4(68, 69, 70, 71)
#define E_S0_2 EXP4(72, 73, 74, 75)
#define E_S0_3 EXP4(76, 77, 78, 79)
#define E_S1_0 EXP4(80, 81, 82, 83)
#define E_S1_1 EXP4(84, 85, 86, 87)
#define E_S1_2 EXP4(88, 89, 90, 91)
#define E_S1_3 EXP4(92, 93, 94, 95)
#define E_S2_0 EXP4(96, 97, 98, 99)
#define E_S2_1 EXP4(100, 101, 102, 103)
#define E_S2_2 EXP4(104, 105, 106, 107)
#define E_S2_3 EXP4(108, 109, 110, 111)
#define E_S3_0 EXP4(112, 113, 114, 115)
#define E_S3_1 EXP4(116, 117, 118, 119)
#define E_S3_2 EXP4(120, 121, 122, 123)
#define E_S3_3 EXP4(124, 125, 126, 127)
#define R_S0 "v[64:79]"
#define R_S1 "v[80:95]"
#define R_S2 "v[96:111]"
#define R_S3 "v[112:127]"
#define R_S0L "v[64:71]"
#define R_S0H "v[72:79]"
#define R_S1L "v[80:87]"
#define R_S1H "v[88:95]"
#define R_S2L "v[96:103]"
#define R_S2H "v[104:111]"
#define R_S3L "v[112:119]"
#define R_S3H "v[120:127]"
#define R_O0 "v[128:143]"
#define R_O1 "v[144:159]"
#define R_Q0M "v[160:163]"
#define R_Q0R "v[164:165]"
#define R_Q1M "v[166:169]"
#define R_Q1R "v[170:171]"
#define R_K0M "v[172:175]"
#define R_K0R "v[176:177]"
#define R_K1M "v[178:181]"
#define R_K1R "v[182:183]"
#define R_K2M "v[184:187]"
#define R_K2R "v[188:189]"
#define R_K3M "v[190:193]"
#define R_K3R "v[194:195]"
#define R_V0A "v[196:199]"
#define R_V0B "v[200:203]"
#define R_V1A "v[204:207]"
#define R_V1B "v[208:211]"
#define R_V2A "v[212:215]"
#define R_V2B "v[216:219]"
#define R_V3A "v[220:223]"
#define R_V3B "v[224:227]"
#define QK1(s, k, q) "v_mfma_f32_32x32x16_bf16 " R_S##s ", " R_K##k##M ", " R_Q##q##M ", 0\n\t"
#define QK2(s, k, q) "v_mfma_f32_32x32x8_bf16 " R_S##s ", " R_K##k##R ", " R_Q##q##R ", " R_S##s "\n\t"
#define PV1(o, v, s) "v_smfmac_f32_32x32x32_bf16 " R_O##o ", " R_V##v##A ", " R_S##s##L ", v228\n\t"
#define PV2(o, v, s) "v_smfmac_f32_32x32x32_bf16 " R_O##o ", " R_V##v##B ", " R_S##s##H ", v228\n\t"
#define PERIOD(e, sn, k, q, sp, v, o) \
    QK1(sn, k, q) E_S##e##_0 PV1(o, v, sp) E_S##e##_1 QK2(sn, k, q) E_S##e##_2 PV2(o, v, sp) E_S##e##_3
#define LDK(k) \
    "global_load_dwordx4 " R_K##k##M ", v229, %[base] sc1\n\t" \
    "s_mov_b32 exec_hi, 0\n\t" \
    "global_load_dwordx2 " R_K##k##R ", v232, %[base] sc1\n\t"
#define LDV(v) \
    "s_mov_b64 exec, %[vmask]\n\t" \
    "global_load_dwordx4 " R_V##v##A ", v230, %[base] sc1\n\t" \
    "global_load_dwordx4 " R_V##v##B ", v230, %[base] offset:640 sc1\n\t"
#define ADV "s_mov_b64 exec, -1\n\ts_add_u32 s60, s60, 5120\n\ts_addc_u32 s61, s61, 0\n\t"
#define GROUP(k, v) "s_waitcnt vmcnt(4)\n\t" LDK(k) LDV(v) ADV

DEV void attn_pass(const char* kv, int odd, u32x4 q0m, u32x2 q0r, u32x4 q1m, u32x2 q1r, int lane, f32x16& o0, f32x16& o1) {
    const unsigned vk = 10240 + odd * 1536 + lane * 16;
    const unsigned vr = 10240 + 1024 + (lane & 31) * 16 + odd * 8;
    const unsigned vv = 2560 + odd * 1280 + (lane >> 5) * 320 + (lane & 31) * 16;
    const unsigned vinit = ((lane & 31) == 20) ? 0x3f803f80u : 0u;
    const u64 vmask = 0x000fffff000fffffull;
    const char* base = kv - 2 * 5120;
    unsigned cnt;
    asm volatile(
        "v_mov_b32 v228, 0xdddd\n\t"
        ".irp r,112,113,114,115,116,117,118,119,120,121,122,123,124,125,126,127,128,129,130,131,132,133,134,135,136,137,138,139,140,141,142,143,144,145,146,147,148,149,150,151,152,153,154,155,156,157,158,159\n\tv_mov_b32 v\\r, 0\n\t.endr\n\t"
        ".irp r,196,197,198,199,200,201,202,203,204,205,206,207,208,209,210,211,212,213,214,215,216,217,218,219,220,221,222,223,224,225,226,227\n\tv_mov_b32 v\\r, %[vinit]\n\t.endr\n\t"
        ".irp r,176,182,188,194\n\tv_mov_b32 v\\r, 0x3f803f80\n\t.endr\n\t"
        ".irp r,177,183,189,195\n\tv_mov_b32 v\\r, 0x3f80\n\t.endr\n\t"
        LDK(0) ADV LDK(1) ADV LDK(2) LDV(0) ADV LDK(3) LDV(1) ADV
        "s_mov_b32 %[cnt], 8\n\t"
        "s_waitcnt vmcnt(10)\n\t"
        QK1(0, 0, 0) QK1(1, 0, 1) QK2(0, 0, 0) QK2(1, 0, 1)
        "s_nop 15\n\t"
        "1:\n\t"
        GROUP(0, 2) PERIOD(0, 2, 1, 0, 3, 3, 1) PERIOD(1, 3, 1, 1, 0, 0, 0)
        GROUP(1, 3) PERIOD(2, 0, 2, 0, 1, 0, 1) PERIOD(3, 1, 2, 1, 2, 1, 0)
        GROUP(2, 0) PERIOD(0, 2, 3, 0, 3, 1, 1) PERIOD(1, 3, 3, 1, 0, 2, 0)
        GROUP(3, 1) PERIOD(2, 0, 0, 0, 1, 2, 1) PERIOD(3, 1, 0, 1, 2, 3, 0)
        "s_sub_u32 %[cnt], %[cnt], 1\n\t"
        "s_cmp_lg_u32 %[cnt], 0\n\t"
        "s_cbranch_scc1 1b\n\t"
        "s_nop 1\n\t"
        PV1(1, 3, 3) PV2(1, 3, 3)
        "s_waitcnt vmcnt(0)\n\t"
        "s_nop 15\n\t"
        : "={v[128:143]}"(o0), "={v[144:159]}"(o1), [base] "+{s[60:61]}"(base), [cnt] "=&s"(cnt)
        : "{v[160:163]}"(q0m), "{v[164:165]}"(q0r), "{v[166:169]}"(q1m), "{v[170:171]}"(q1r), "{v229}"(vk), "{v230}"(vv), "{v232}"(vr),
          [vinit] "{v231}"(vinit), [vmask] "s"(vmask)
        : "memory", "scc",
          "v64", "v65", "v66", "v67", "v68", "v69", "v70", "v71", "v72", "v73", "v74", "v75", "v76", "v77", "v78", "v79",
          "v80", "v81", "v82", "v83", "v84", "v85", "v86", "v87", "v88", "v89", "v90", "v91", "v92", "v93", "v94", "v95",
          "v96", "v97", "v98", "v99", "v100", "v101", "v102", "v103", "v104", "v105", "v106", "v107", "v108", "v109", "v110", "v111",
          "v112", "v113", "v114", "v115", "v116", "v117", "v118", "v119", "v120", "v121", "v122", "v123", "v124", "v125", "v126", "v127",
          "v172", "v173", "v174", "v175", "v176", "v177", "v178", "v179", "v180", "v181", "v182", "v183", "v184", "v185", "v186", "v187",
          "v188", "v189", "v190", "v191", "v192", "v193", "v194", "v195", "v196", "v197", "v198", "v199", "v200", "v201", "v202", "v203",
          "v204", "v205", "v206", "v207", "v208", "v209", "v210", "v211", "v212", "v213", "v214", "v215", "v216", "v217", "v218", "v219",
          "v220", "v221", "v222", "v223", "v224", "v225", "v226", "v227", "v228");
}

DEV float max16(const f32x16& s) {
    float a = fmaxf(fmaxf(s[0], s[1]), s[2]), b = fmaxf(fmaxf(s[3], s[4]), s[5]);
    a = fmaxf(fmaxf(a, s[6]), s[7]); b = fmaxf(fmaxf(b, s[8]), s[9]);
    a = fmaxf(fmaxf(a, s[10]), s[11]); b = fmaxf(fmaxf(b, s[12]), s[13]);
    a = fmaxf(fmaxf(a, s[14]), s[15]);
    return fmaxf(a, b);
}

struct Ring {
    const char *x, *wq, *wgu;
    unsigned xo0, xo1, wo0, wo1, go0, go1, go2;
    unsigned lds0; int w;
};
template <int C> DEV void ring_issue(const Ring& r) {
    if constexpr (C < 10) {
        const unsigned d = r.lds0 + (C & 3) * 32768 + r.w * 2048;
        glds16(r.xo0, r.x + C * 128, d); glds16(r.xo1, r.x + C * 128, d + 1024);
        glds16(r.wo0, r.wq + C * 64 * 3840, d + 16384); glds16(r.wo1, r.wq + C * 64 * 3840, d + 16384 + 1024);
    } else if constexpr (C < 20) {
        const unsigned d = r.lds0 + (C & 3) * 32768 + r.w * 3072 + (r.w >> 2) * 32;
        const char* b = r.wgu + (C - 10) * 64 * 3072;
        glds16(r.go0, b, d); glds16(r.go1, b, d + 1024); glds16(r.go2, b, d + 2048);
    }
}
constexpr int ring_n(int c) { return c < 10 ? 4 : (c < 20 ? 3 : 0); }

template <int C> DEV void a1_chunk(const Ring& r, const LAS char* lds, unsigned xa, unsigned wa0, unsigned wa1,
                                    bf16x8 (&xf)[40], f32x16 (&acc)[2], float& rs, float& rq, float& cs0, float& cs1) {
    wait_bar<ring_n(C + 1) + ring_n(C + 2)>();
    ring_issue<C + 3>(r);
    const LAS char* s = lds + (C & 3) * 32768;
#pragma unroll
    for (int ks = 0; ks < 4; ++ks) {
        const bf16x8 xv = lds128(s + (xa ^ (ks << 5)));
        xf[4 * C + ks] = xv;
        const bf16x8 w0 = ldstr(s + (wa0 ^ (ks << 12)), s + (wa0 ^ (ks << 12)) + 1024);
        const bf16x8 w1 = ldstr(s + (wa1 ^ (ks << 12)), s + (wa1 ^ (ks << 12)) + 1024);
        acc[0] = MFMA32(w0, xv, acc[0]); acc[1] = MFMA32(w1, xv, acc[1]);
        if (ks < 2) { rs = dot_ones(xv, rs); rq = dot_self(xv, rq); }
        if (ks == 0) { cs0 = dot_ones(w0, cs0); cs1 = dot_ones(w1, cs1); }
    }
}
template <int C> DEV void a2_chunk(const Ring& r, const LAS char* lds, const unsigned (&ga)[4], const bf16x8 (&xf)[40], f32x16 (&acc)[3], float (&cs)[3]) {
    wait_bar<ring_n(C + 1) + ring_n(C + 2) + (C < 13 ? 4 : 0)>();
    ring_issue<C + 3>(r);
    const LAS char* s = lds + (C & 3) * 32768;
#pragma unroll
    for (int ks = 0; ks < 4; ++ks) {
#pragma unroll
        for (int b = 0; b < 3; ++b) {
            const bf16x8 wv = ldstr(s + ga[ks] + b * 32, s + ga[ks] + b * 32 + 768);
            acc[b] = MFMA32(wv, xf[4 * (C - 10) + ks], acc[b]);
            if (ks == 0) cs[b] = dot_ones(wv, cs[b]);
        }
    }
}

template <int PH>
__global__ __launch_bounds__(512, 2) void fused_block(const char* __restrict__ x, const char* __restrict__ wqkv, const char* __restrict__ wo,
                                                      const char* __restrict__ wg, const char* __restrict__ wu, const char* __restrict__ wd,
                                                      char* __restrict__ out, char* ws) {
    __shared__ __attribute__((aligned(16))) char smem[L_TOTAL];
    LAS char* lds = (LAS char*)smem;
    const int w = __builtin_amdgcn_readfirstlane(threadIdx.x >> 6);
    const int bid = blockIdx.x, xcd = bid & 7, rr = bid >> 3;
#define LANE_IDS int lane = __builtin_amdgcn_mbcnt_hi(~0u, __builtin_amdgcn_mbcnt_lo(~0u, 0u)); asm volatile("" : "+v"(lane)); \
    const int tid = 64 * w + lane, r32 = lane & 31, hh = lane >> 5; (void)tid; (void)r32; (void)hh
    const __amdgpu_buffer_rsrc_t wsr = __builtin_amdgcn_make_buffer_rsrc(ws, 0, WS_END, 0x00020000);
    u64 tag = 0;
    STAMP_DECL;
    STAMP(0);

    if constexpr (PH & 1) {
        LANE_IDS;
        const int m = 8 * (xcd & 1) + (rr & 7), j = 4 * (xcd >> 1) + (rr >> 3), jq = j < 15 ? j : 14;
        const int wm = w & 3, wn = w >> 2;
        Ring rg;
        rg.x = x + m * 128 * 1280; rg.wq = wqkv + jq * 256; rg.wgu = (w < 4 ? wg : wu) + j * 192;
        rg.lds0 = (unsigned)(size_t)lds; rg.w = w;
        rg.xo0 = (16 * w + (lane >> 3)) * 1280 + (((lane & 7) ^ (lane >> 4)) << 4);
        rg.xo1 = (16 * w + 8 + (lane >> 3)) * 1280 + (((lane & 7) ^ (4 + (lane >> 4))) << 4);
        rg.wo0 = (8 * w + (lane >> 4)) * 3840 + (((lane & 15) ^ ((lane >> 4) << 2)) << 4);
        rg.wo1 = rg.wo0 + 4 * 3840;
        { const int p0 = (w & 3) * 192 + lane, p1 = p0 + 64, p2 = p0 + 128;
          rg.go0 = (p0 / 12) * 3072 + (p0 % 12) * 16; rg.go1 = (p1 / 12) * 3072 + (p1 % 12) * 16; rg.go2 = (p2 / 12) * 3072 + (p2 % 12) * 16; }
        ring_issue<0>(rg); ring_issue<1>(rg); ring_issue<2>(rg);
        if constexpr (FUSED) {
            asm volatile("s_load_dwordx2 %0, %1, 0x0\n\ts_waitcnt lgkmcnt(0)" : "=s"(tag) : "s"(ws) : "memory");
            tag += 1;
        }

        const int g4 = lane >> 4, q4 = (lane >> 2) & 3, p4 = lane & 3, rot = wm ^ (2 * wn);
        const int xrow = 32 * wm + r32;
        const unsigned xa = (xrow * 128 + ((hh ^ ((xrow >> 1) & 7)) << 4)) ^ (rot << 5);
        const unsigned wab = 16384 + (rot << 12) + (8 * (g4 >> 1) + q4) * 256 + (g4 & 1) * 32 + (p4 & 1) * 16 + (p4 >> 1) * 8;
        const unsigned wa0 = wab + (((2 * wn) ^ q4) << 6), wa1 = wab + (((2 * wn + 1) ^ q4) << 6);
        bf16x8 xf[40];
        f32x16 acc[2] = {};
        float rs = 0.f, rq = 0.f, cs0 = 0.f, cs1 = 0.f;
#define A1C(c) a1_chunk<c>(rg, lds, xa, wa0, wa1, xf, acc, rs, rq, cs0, cs1)
        A1C(0); A1C(1); A1C(2); A1C(3); A1C(4); A1C(5); A1C(6); A1C(7); A1C(8); A1C(9);
#undef A1C
        STAMP(1);
        LAS float* rowp = (LAS float*)(lds + L_ROWP); LAS float* colp = (LAS float*)(lds + L_COLP); LAS float* priv = (LAS float*)(lds + L_PRIV) + w * 128;
        rs = swap_add(rs); rq = swap_add(rq); cs0 = swap_add(cs0); cs1 = swap_add(cs1);
        rowp[(wn * 128 + xrow) * 2 + hh] = hh ? rq : rs;
        colp[wm * 192 + 64 * wn + lane] = hh ? cs1 : cs0;
        lds_bar();
        float mu, rstd;
        { const float s = rowp[xrow * 2] + rowp[(128 + xrow) * 2], q = rowp[xrow * 2 + 1] + rowp[(128 + xrow) * 2 + 1];
          const float mean = s * (1.f / 640.f), var = fmaxf(q * (1.f / 640.f) - mean * mean, 0.f);
          mu = bfr(mean); rstd = 1.f / bfr(sqrtf(bfr(bfr(var) + 1e-5f))); }
        priv[lane] = colp[64 * wn + lane] + colp[192 + 64 * wn + lane] + colp[384 + 64 * wn + lane] + colp[576 + 64 * wn + lane];
        asm volatile("s_waitcnt lgkmcnt(0)" ::: "memory");
        { const int tile = 4 * m + wm;
          const float sc = j < 5 ? rstd * QSCALE : rstd;
          u32x4 y[4];
#pragma unroll
          for (int b = 0; b < 2; ++b)
#pragma unroll
              for (int u = 0; u < 4; ++u) {
                  const f32x4 c = *(const LAS f32x4*)(priv + 32 * b + 8 * u + 4 * hh);
                  y[2 * b + (u >> 1)][2 * (u & 1)] = pk2(sc * (acc[b][4 * u] - mu * c[0]), sc * (acc[b][4 * u + 1] - mu * c[1]));
                  y[2 * b + (u >> 1)][2 * (u & 1) + 1] = pk2(sc * (acc[b][4 * u + 2] - mu * c[2]), sc * (acc[b][4 * u + 3] - mu * c[3]));
              }
          if (j < 10) {
              const unsigned base = (j < 5 ? WS_Q + tile * 2560 : WS_KV + tile * 5120) + r32 * 16, ps = j < 5 ? 64 * 2560 : 64 * 5120;
              const int c8 = (j < 5 ? j : j - 5) * 16 + 8 * wn + hh;
#pragma unroll
              for (int i = 0; i < 4; ++i) {
                  const int ci = c8 + 2 * i, pr = ci / 5, pl = ci - 5 * pr;
                  __builtin_amdgcn_raw_buffer_store_b128(y[i], wsr, base + pr * ps + pl * 512, 0, SC1);
              }
          } else {
              LAS char* tp = lds + 32768 + w * 4096;
#pragma unroll
              for (int i = 0; i < 4; ++i) *(LAS u32x4*)(tp + r32 * 128 + (((2 * i + hh) ^ (r32 & 7)) << 4)) = y[i];
              asm volatile("s_waitcnt lgkmcnt(0)" ::: "memory");
              const int col = (j - 10) * 128 + 64 * wn + lane, head = col / 20, d = col - 20 * head;
              const unsigned base = j < 15 ? WS_KV + ((head >> 1) * 64 + tile) * 5120 + 2560 + (head & 1) * 1280 + d * 16 : OOB;
              const int cu = 2 * g4 + (p4 >> 1);
#pragma unroll
              for (int u = 0; u < 4; ++u) {
                  const bf16x8 v = ldstr(tp + (8 * u + q4) * 128 + ((cu ^ q4) << 4) + (p4 & 1) * 8,
                                         tp + (8 * u + 4 + q4) * 128 + ((cu ^ (4 + q4)) << 4) + (p4 & 1) * 8);
                  __builtin_amdgcn_raw_buffer_store_b128(__builtin_bit_cast(u32x4, v), wsr, base + u * 320, 0, SC1);
              }
          } }
        STAMP(2);
        unsigned ga[4];
#pragma unroll
        for (int ks = 0; ks < 4; ++ks) ga[ks] = (g4 & 1) * (12288 + 32) + (8 * (g4 >> 1) + q4) * 192 + wn * 96 + p4 * 8 + (ks ^ rot) * 3072;
        f32x16 ac2[3] = {};
        float cs[3] = {0.f, 0.f, 0.f};
#define A2C(c) a2_chunk<c>(rg, lds, ga, xf, ac2, cs)
        A2C(10); A2C(11); A2C(12); A2C(13);
        if constexpr (FUSED) {
            if (tid == 0) __hip_atomic_store((GAS u64*)(ws + WS_FA) + bid, tag, __ATOMIC_RELAXED, __HIP_MEMORY_SCOPE_AGENT);
        }
        A2C(14); A2C(15); A2C(16); A2C(17); A2C(18); A2C(19);
#undef A2C
        STAMP(3);
        cs[0] = swap_add(cs[0]); cs[1] = swap_add(cs[1]); cs[2] = swap_add(cs[2]);
        colp[wm * 192 + 96 * wn + lane] = hh ? cs[1] : cs[0];
        if (hh == 0) colp[wm * 192 + 96 * wn + 64 + lane] = cs[2];
        lds_bar();
        { const int i0 = 96 * wn + lane, i1 = 96 * wn + 64 + r32;
          priv[lane] = colp[i0] + colp[192 + i0] + colp[384 + i0] + colp[576 + i0];
          priv[64 + r32] = colp[i1] + colp[192 + i1] + colp[384 + i1] + colp[576 + i1]; }
        asm volatile("s_waitcnt lgkmcnt(0)" ::: "memory");
        { const unsigned base = WS_HP + (8 * m + 2 * wm + (r32 >> 4)) * (272 * 256) + (2 * (6 * j + 3 * wn) + hh) * 256 + (r32 & 15) * 16;
#pragma unroll
          for (int b = 0; b < 3; ++b) {
              u32x4 v;
#pragma unroll
              for (int u = 0; u < 2; ++u) {
                  const f32x4 cg = *(const LAS f32x4*)(priv + 32 * b + 8 * u + 4 * hh), cu = *(const LAS f32x4*)(priv + 32 * b + 16 + 8 * u + 4 * hh);
                  float hv[4];
#pragma unroll
                  for (int i = 0; i < 4; ++i)
                      hv[i] = bfr(rstd * (ac2[b][4 * u + i] - mu * cg[i])) * bfr(rstd * (ac2[b][8 + 4 * u + i] - mu * cu[i]));
                  v[2 * u] = pk2(hv[0], hv[1]); v[2 * u + 1] = pk2(hv[2], hv[3]);
              }
              __builtin_amdgcn_raw_buffer_store_b128(v, wsr, base + b * 512, 0, SC1);
          } }
        { const int unit = bid + 256 * w;
          if (unit < 680) {
              const int s = unit / 10, nb = unit - 10 * s, kq = lane >> 4, n4 = lane & 15, kap = 4 * s + kq;
              const char* src; int r0, r1;
              if (s < 48) { src = wd; r0 = 16 * (kap >> 1) + 4 * (kap & 1); r1 = r0 + 8; }
              else if (s < 64) { src = wo; r0 = 20 * ((kap - 192) >> 1) + 4 * (kap & 1); r1 = r0 + 8; }
              else { src = wo; r0 = 40 * (kap - 256) + 16; r1 = r0 + 20; }
              u32x2 a[8];
#pragma unroll
              for (int i = 0; i < 4; ++i) { a[i] = *(const u32x2*)(src + (r0 + i) * 1280 + nb * 128 + n4 * 8); a[4 + i] = *(const u32x2*)(src + (r1 + i) * 1280 + nb * 128 + n4 * 8); }
              const unsigned base = WS_WP + ((4 * nb + (n4 >> 2)) * 68 + s) * 1024 + (16 * kq + 4 * (n4 & 3)) * 16;
#pragma unroll
              for (int i = 0; i < 4; ++i) {
                  u32x4 v; const unsigned sel = (i & 1) ? 0x07060302u : 0x05040100u;
#pragma unroll
                  for (int e = 0; e < 4; ++e) v[e] = __builtin_amdgcn_perm(a[2 * e + 1][i >> 1], a[2 * e][i >> 1], sel);
                  __builtin_amdgcn_raw_buffer_store_b128(v, wsr, base + i * 16, 0, SC1);
              }
          } }
        if constexpr (FUSED) {
            wait_bar<0>();
            if (w == 0) {
                if (lane == 0) __hip_atomic_store((GAS u64*)(ws + WS_FH) + bid, tag, __ATOMIC_RELAXED, __HIP_MEMORY_SCOPE_AGENT);
                poll_flags<4>(ws, WS_FA + lane * 8, 512, true, tag);
            }
            lds_bar();
        }
        STAMP(4);
    }

    if constexpr (PH & 2) {
        LANE_IDS;
        const int g = 4 * xcd + (rr >> 3), mq = rr & 7, qq = w & 3, kh = w >> 2;
        const int odd = g & 1;
        const unsigned mo = odd * 1536 + lane * 16, ro = 1024 + r32 * 16 + odd * 8;
        const unsigned qoff = WS_Q + ((g >> 1) * 64 + 8 * mq + 2 * qq) * 2560;
        const unsigned kvoff = WS_KV + ((g >> 1) * 64 + 32 * kh) * 5120;
        u32x4 qm[2]; u32x2 qr[2];
#pragma unroll
        for (int i = 0; i < 2; ++i) {
            qm[i] = __builtin_amdgcn_raw_buffer_load_b128(wsr, mo, qoff + i * 2560, SC1);
            qr[i] = __builtin_amdgcn_raw_buffer_load_b64(wsr, ro, qoff + i * 2560, SC1);
        }
        LAS float* xch = (LAS float*)lds;
        LAS int* bad = (LAS int*)(lds + L_FLAG);
        if (tid == 0) *bad = 0;
        for (int attempt = 0; attempt < 2; ++attempt) {
            float c[2] = {-INFINITY, -INFINITY};
            if (hh) { qr[0] = (u32x2){0u, 0u}; qr[1] = (u32x2){0u, 0u}; }
            const int nt = attempt ? 32 : 1;
            for (int t = 0; t < nt; ++t) {
                const u32x4 km = __builtin_amdgcn_raw_buffer_load_b128(wsr, mo, kvoff + t * 5120, SC1);
                u32x2 kr = __builtin_amdgcn_raw_buffer_load_b64(wsr, ro, kvoff + t * 5120, SC1);
                if (hh) kr = (u32x2){0u, 0u};
#pragma unroll
                for (int i = 0; i < 2; ++i) {
                    f32x16 s = MFMA32(__builtin_bit_cast(bf16x8, km), __builtin_bit_cast(bf16x8, qm[i]), (f32x16){});
                    s = __builtin_amdgcn_mfma_f32_32x32x8bf16_1k(__builtin_bit_cast(s16x4, kr), __builtin_bit_cast(s16x4, qr[i]), s, 0, 0, 0);
                    c[i] = fmaxf(c[i], max16(s));
                }
            }
#pragma unroll
            for (int i = 0; i < 2; ++i) {
                c[i] = swap_max(c[i]);
                const float c0 = bfr(c[i]), c1 = bfr(c[i] - c0), c2 = bfr(c[i] - c0 - c1);
                c[i] = c0 + c1 + c2;
                if (hh) qr[i] = (u32x2){pk2(-c0, -c1), pk2(-c2, 0.f)};
            }
            f32x16 o[2];
            STAMP(5);
            attn_pass(ws + kvoff, odd, qm[0], qr[0], qm[1], qr[1], lane, o[0], o[1]);
            STAMP(6);
            { LAS float* snd = xch + w * 13 * 64 + lane;
#pragma unroll
              for (int rdx = 0; rdx < 12; ++rdx) snd[rdx * 64] = kh ? o[0][rdx] : o[1][rdx];
              snd[12 * 64] = kh ? c[0] : c[1]; }
            lds_bar();
            float f[12];
            { const LAS float* rcv = xch + (w ^ 4) * 13 * 64 + lane;
              const float cm = kh ? c[1] : c[0], co = rcv[12 * 64], cx = fmaxf(cm, co);
              const float wa = __builtin_amdgcn_exp2f(cm - cx), wb = __builtin_amdgcn_exp2f(co - cx);
#pragma unroll
              for (int rdx = 0; rdx < 12; ++rdx) f[rdx] = (kh ? o[1][rdx] : o[0][rdx]) * wa + rcv[rdx * 64] * wb; }
            const float l = swap_add(hh ? f[8] : 0.f);
            float chk = l;
#pragma unroll
            for (int rdx = 0; rdx < 8; ++rdx) chk += fabsf(f[rdx]);
            if (!hh) chk += fabsf(f[8]) + fabsf(f[9]) + fabsf(f[10]) + fabsf(f[11]);
            if (!(chk < 3.0e38f) || !(l > 0.f)) *bad = 1;
            const float inv = 1.f / l;
            { const unsigned base = WS_HP + (16 * mq + 4 * qq + 2 * kh + (r32 >> 4)) * (272 * 256) + (r32 & 15) * 16;
              const u32x4 v = {pk2(f[0] * inv, f[1] * inv), pk2(f[2] * inv, f[3] * inv), pk2(f[4] * inv, f[5] * inv), pk2(f[6] * inv, f[7] * inv)};
              __builtin_amdgcn_raw_buffer_store_b128(v, wsr, base + (192 + 2 * g + hh) * 256, 0, SC1);
              const u32x2 v2 = {pk2(f[8] * inv, f[9] * inv), pk2(f[10] * inv, f[11] * inv)};
              __builtin_amdgcn_raw_buffer_store_b64(v2, wsr, hh ? OOB : base + (256 + (g >> 1)) * 256 + (g & 1) * 8, 0, SC1); }
            wait_bar<0>();
            if (__builtin_amdgcn_readfirstlane(*bad) == 0) break;
        }
        if constexpr (FUSED) {
            if (w == 0) {
                if (lane == 0) __hip_atomic_store((GAS u64*)(ws + WS_FO + (mq * 32 + g) * 128), tag, __ATOMIC_RELAXED, __HIP_MEMORY_SCOPE_AGENT);
                poll_flags<4>(ws, WS_FH + lane * 8, 512, true, tag);
                if (bid == 0 && lane == 0) __hip_atomic_store((GAS u64*)(ws + WS_EPOCH), tag, __ATOMIC_RELAXED, __HIP_MEMORY_SCOPE_AGENT);
            }
            lds_bar();
        }
        STAMP(7);
    }

    if constexpr (PH & 4) {
        LANE_IDS;
        const int rb = 4 * xcd + (rr >> 3), ct = rr & 7;
        const unsigned ao = WS_HP + 4 * rb * (272 * 256) + lane * 16, bo = WS_WP + 5 * ct * (68 * 1024) + lane * 16;
        f32x4 acc[4][5] = {};
        auto load = [&](int s, u32x4 (&a)[4], u32x4 (&b)[5]) {
#pragma unroll
            for (int i = 0; i < 4; ++i) a[i] = __builtin_amdgcn_raw_buffer_load_b128(wsr, ao + i * (272 * 256), s * 1024, SC1);
#pragma unroll
            for (int i = 0; i < 5; ++i) b[i] = __builtin_amdgcn_raw_buffer_load_b128(wsr, bo + i * (68 * 1024), s * 1024, SC1);
        };
        auto mma = [&](const u32x4 (&a)[4], const u32x4 (&b)[5]) {
#pragma unroll
            for (int i = 0; i < 4; ++i)
#pragma unroll
                for (int n = 0; n < 5; ++n) acc[i][n] = MFMA16(__builtin_bit_cast(bf16x8, b[n]), __builtin_bit_cast(bf16x8, a[i]), acc[i][n]);
        };
        u32x4 a0[4], b0[5], a1[4], b1[5], a2[4], b2[5];
        load(w, a0, b0); load(w + 8, a1, b1); load(w + 16, a2, b2);
        mma(a0, b0); load(w + 24, a0, b0);
        mma(a1, b1); load(w + 32, a1, b1);
        mma(a2, b2); load(w + 40, a2, b2);
        mma(a0, b0); mma(a1, b1); mma(a2, b2);
        STAMP(8);
        if constexpr (FUSED) {
            if (w == 0) poll_flags<1>(ws, WS_FO + ((rb >> 2) * 32 + r32) * 128, 0, true, tag);
            lds_bar();
        }
        STAMP(9);
        load(48 + w, a0, b0); load(56 + w, a1, b1);
        if (w < 4) load(64 + w, a2, b2);
        mma(a0, b0); mma(a1, b1);
        if (w < 4) mma(a2, b2);
        STAMP(10);
        LAS f32x4* red = (LAS f32x4*)lds;
#pragma unroll
        for (int i = 0; i < 4; ++i)
#pragma unroll
            for (int n = 0; n < 5; ++n) red[w * 1280 + (i * 5 + n) * 64 + lane] = acc[i][n];
        lds_bar();
#pragma unroll
        for (int it = 0; it < 3; ++it) {
            const int item = tid + 512 * it;
            if (item < 1280) {
                f32x4 s = red[item];
#pragma unroll
                for (int k = 1; k < 8; ++k) s += red[k * 1280 + item];
                const int tl = item >> 6, i = tl / 5, n = tl - 5 * i;
                const int off = (64 * rb + 16 * i + (lane & 15)) * 1280 + (80 * ct + 16 * n + 4 * (lane >> 4)) * 2;
                const u32x2 xv = *(const u32x2*)(x + off);
                u32x2 v;
                v[0] = pk2(s[0] + __uint_as_float(xv[0] << 16), s[1] + __uint_as_float(xv[0] & 0xffff0000u));
                v[1] = pk2(s[2] + __uint_as_float(xv[1] << 16), s[3] + __uint_as_float(xv[1] & 0xffff0000u));
                *(u32x2*)(out + off) = v;
            }
        }
        STAMP(11);
    }
    STAMP_OUT;
}

extern "C" void kernel_launch(void* const* d_in, const int* in_sizes, int n_in, void* d_out, int out_size, void* d_ws, size_t ws_size,
                              hipStream_t stream) {
    const char* x = (const char*)d_in[0]; const char* wqkv = (const char*)d_in[1]; const char* wo = (const char*)d_in[2];
    const char* wg = (const char*)d_in[4]; const char* wu = (const char*)d_in[5]; const char* wd = (const char*)d_in[6];
    fused_block<7><<<256, 512, 0, stream>>>(x, wqkv, wo, wg, wu, wd, (char*)d_out, (char*)d_ws);
}
```

```cpp
#include <hip/hip_runtime.h>
#include <hip/hip_bf16.h>

#define STAMPS 0

typedef __bf16 bf16x8 __attribute__((ext_vector_type(8)));
typedef __bf16 bf16x2 __attribute__((ext_vector_type(2)));
typedef short s16x4 __attribute__((ext_vector_type(4)));
typedef float f32x32 __attribute__((ext_vector_type(32)));
typedef float f32x16 __attribute__((ext_vector_type(16)));
typedef float f32x4 __attribute__((ext_vector_type(4)));
typedef unsigned u32x32 __attribute__((ext_vector_type(32)));
typedef unsigned u32x16 __attribute__((ext_vector_type(16)));
typedef unsigned u32x8 __attribute__((ext_vector_type(8)));
typedef unsigned u32x4 __attribute__((ext_vector_type(4)));
typedef unsigned u32x2 __attribute__((ext_vector_type(2)));
typedef unsigned long long u64;
#define LAS __attribute__((address_space(3)))
#define GAS __attribute__((address_space(1)))
#define DEV __device__ __forceinline__

constexpr unsigned WS_EPOCH = 0;
constexpr unsigned WS_FA = 4096;
constexpr unsigned WS_FH = 8192;
constexpr unsigned WS_FO = 16384;
constexpr unsigned WS_GA = 65536;
constexpr unsigned WS_GB = WS_GA + 256 * 384 * 8;
constexpr unsigned WS_Q = WS_GB + 256 * 192 * 8;
constexpr unsigned WS_KV = WS_Q + 16 * 64 * 2560;
constexpr unsigned WS_HP = WS_KV + 16 * 64 * 5120;
constexpr unsigned WS_WP = WS_HP + 128 * 272 * 256;
constexpr unsigned WS_END = WS_WP + 40 * 68 * 1024;
constexpr unsigned WS_STAMP = 32u << 20;
constexpr unsigned OOB = 0x40000000u;

constexpr int L_TR = 131072;
constexpr int L_PART = L_TR + 16384;
constexpr int L_FIN = L_PART + 6144;
constexpr int L_FLAG = L_FIN + 5120;
constexpr int L_TOTAL = 163840;

constexpr float QSCALE = 0.22360679774997896f * 1.4426950408889634f;
constexpr int SC1 = 16;
constexpr unsigned SPIN_CAP = 1u << 22;

DEV unsigned pk2(float lo, float hi) { bf16x2 v = {(__bf16)lo, (__bf16)hi}; return __builtin_bit_cast(unsigned, v); }
DEV float bfr(float x) { return (float)(__bf16)x; }
DEV u32x2 split3(float v) {
    const float a = bfr(v), b = bfr(v - a), c = v - a - b;
    return (u32x2){pk2(a, b), pk2(c, 0.f)};
}
DEV float swap_add(float v) {
    auto r = __builtin_amdgcn_permlane32_swap(__float_as_uint(v), __float_as_uint(v), false, false);
    return __uint_as_float(r[0]) + __uint_as_float(r[1]);
}
DEV float swap_max(float v) {
    auto r = __builtin_amdgcn_permlane32_swap(__float_as_uint(v), __float_as_uint(v), false, false);
    return fmaxf(__uint_as_float(r[0]), __uint_as_float(r[1]));
}
DEV void glds16(unsigned voff, const void* sbase, unsigned ldsdst) {
    asm volatile("s_mov_b32 m0, %2\n\ts_nop 0\n\tglobal_load_lds_dwordx4 %0, %1" ::"v"(voff), "s"(sbase), "s"(ldsdst) : "memory");
}
DEV void glds4(unsigned voff, const void* sbase, unsigned ldsdst) {
    asm volatile("s_mov_b32 m0, %2\n\ts_nop 0\n\tglobal_load_lds_dword %0, %1" ::"v"(voff), "s"(sbase), "s"(ldsdst) : "memory");
}
template <int N> DEV void wait_bar() { asm volatile("s_waitcnt vmcnt(%0) lgkmcnt(0)\n\ts_barrier" ::"i"(N) : "memory"); }
DEV void lds_bar() { asm volatile("s_waitcnt lgkmcnt(0)\n\ts_barrier" ::: "memory"); }
DEV bf16x8 lds128(const LAS char* p) { return *(const LAS bf16x8*)p; }
DEV bf16x8 ldstr(const LAS char* p0, const LAS char* p1) {
    s16x4 a = __builtin_amdgcn_ds_read_tr16_b64_v4i16((LAS s16x4*)p0), b = __builtin_amdgcn_ds_read_tr16_b64_v4i16((LAS s16x4*)p1);
    return __builtin_bit_cast(bf16x8, __builtin_shufflevector(a, b, 0, 1, 2, 3, 4, 5, 6, 7));
}
DEV float dot_ones(bf16x8 v, float acc, unsigned ones = 0x3f803f80u) {
    const bf16x2 one = __builtin_bit_cast(bf16x2, ones);
#pragma unroll
    for (int i = 0; i < 4; ++i) acc = __builtin_amdgcn_fdot2_f32_bf16((bf16x2){v[2 * i], v[2 * i + 1]}, one, acc, false);
    return acc;
}
DEV float dot_self(bf16x8 v, float acc) {
#pragma unroll
    for (int i = 0; i < 4; ++i) { bf16x2 p = {v[2 * i], v[2 * i + 1]}; acc = __builtin_amdgcn_fdot2_f32_bf16(p, p, acc, false); }
    return acc;
}
#define MFMA32(a, b, c) __builtin_amdgcn_mfma_f32_32x32x16_bf16(a, b, c, 0, 0, 0)
#define MFMA8(a, b, c) __builtin_amdgcn_mfma_f32_32x32x8bf16_1k(__builtin_bit_cast(s16x4, a), __builtin_bit_cast(s16x4, b), c, 0, 0, 0)
#define MFMA16(a, b, c) __builtin_amdgcn_mfma_f32_16x16x32_bf16(a, b, c, 0, 0, 0)
#define SBAR() __builtin_amdgcn_sched_barrier(0)

#define STAMP(i) do {} while (0)
#define STAMPP(p, i) do {} while (0)
#define STAMP_DECL
#define STAMP_OUT do {} while (0)

DEV u32x2 ld2_async(const char* base, unsigned off) { u32x2 v; asm volatile("global_load_dwordx2 %0, %1, %2 sc1" : "=v"(v) : "v"(off), "s"(base) : "memory"); return v; }
DEV u32x4 ld4_async(const char* base, unsigned off) { u32x4 v; asm volatile("global_load_dwordx4 %0, %1, %2 sc1" : "=v"(v) : "v"(off), "s"(base) : "memory"); return v; }
DEV void flag_store(char* p, u64 tag) { __hip_atomic_store((GAS u64*)p, tag, __ATOMIC_RELAXED, __HIP_MEMORY_SCOPE_AGENT); }
DEV float gran_sum(const char* ws, unsigned off, unsigned stride, u32x2 (&g)[4], unsigned tag) {
#pragma unroll 1
    for (unsigned spin = 0; spin < SPIN_CAP; ++spin) {
        if (__all(g[0][1] == tag && g[1][1] == tag && g[2][1] == tag && g[3][1] == tag)) break;
        asm volatile("s_sleep 1\n\tglobal_load_dwordx2 %0, %4, %8 sc1\n\tglobal_load_dwordx2 %1, %5, %8 sc1\n\t"
                     "global_load_dwordx2 %2, %6, %8 sc1\n\tglobal_load_dwordx2 %3, %7, %8 sc1\n\ts_waitcnt vmcnt(0)"
                     : "=&v"(g[0]), "=&v"(g[1]), "=&v"(g[2]), "=&v"(g[3])
                     : "v"(off), "v"(off + stride), "v"(off + 2 * stride), "v"(off + 3 * stride), "s"(ws) : "memory");
    }
    return (__uint_as_float(g[0][0]) + __uint_as_float(g[1][0])) + (__uint_as_float(g[2][0]) + __uint_as_float(g[3][0]));
}
DEV void flag_wait(const char* ws, unsigned off, u32x2 f, u64 tag) {
#pragma unroll 1
    for (unsigned spin = 0; spin < SPIN_CAP; ++spin) {
        if (__all(f[0] == (unsigned)tag && f[1] == (unsigned)(tag >> 32))) break;
        asm volatile("s_sleep 1\n\tglobal_load_dwordx2 %0, %1, %2 sc1\n\ts_waitcnt vmcnt(0)" : "=&v"(f) : "v"(off), "s"(ws) : "memory");
    }
}
DEV void flag_wait4(const char* ws, unsigned off, u32x4 f, u32x4 g, u64 tag) {
    const unsigned lo = (unsigned)tag, hi = (unsigned)(tag >> 32);
#pragma unroll 1
    for (unsigned spin = 0; spin < SPIN_CAP; ++spin) {
        if (__all(f[0] == lo && f[1] == hi && f[2] == lo && f[3] == hi && g[0] == lo && g[1] == hi && g[2] == lo && g[3] == hi)) break;
        asm volatile("s_sleep 1\n\tglobal_load_dwordx4 %0, %2, %3 sc1\n\tglobal_load_dwordx4 %1, %2, %3 offset:1024 sc1\n\ts_waitcnt vmcnt(0)"
                     : "=&v"(f), "=&v"(g) : "v"(off), "s"(ws) : "memory");
    }
}
DEV void arrive_publish(LAS unsigned* cnt, int lane, char* flag, u64 tag) {
    if (lane == 0) {
        const unsigned old = __hip_atomic_fetch_add(cnt, 1u, __ATOMIC_RELAXED, __HIP_MEMORY_SCOPE_WORKGROUP);
        if (old == 7u) flag_store(flag, tag);
    }
}

#define EXP4(a, b, c, d) "v_exp_f32 v" #a ", v" #a "\n\tv_exp_f32 v" #b ", v" #b "\n\tv_exp_f32 v" #c ", v" #c "\n\tv_exp_f32 v" #d ", v" #d "\n\t"
#define E_S0_0 EXP4(64, 65, 66, 67)
#define E_S0_1 EXP4(68, 69, 70, 71)
#define E_S0_2 EXP4(72, 73, 74, 75)
#define E_S0_3 EXP4(76, 77, 78, 79)
#define E_S1_0 EXP4(80, 81, 82, 83)
#define E_S1_1 EXP4(84, 85, 86, 87)
#define E_S1_2 EXP4(88, 89, 90, 91)
#define E_S1_3 EXP4(92, 93, 94, 95)
#define E_S2_0 EXP4(96, 97, 98, 99)
#define E_S2_1 EXP4(100, 101, 102, 103)
#define E_S2_2 EXP4(104, 105, 106, 107)
#define E_S2_3 EXP4(108, 109, 110, 111)
#define E_S3_0 EXP4(112, 113, 114, 115)
#define E_S3_1 EXP4(116, 117, 118, 119)
#define E_S3_2 EXP4(120, 121, 122, 123)
#define E_S3_3 EXP4(124, 125, 126, 127)
#define R_S0 "v[64:79]"
#define R_S1 "v[80:95]"
#define R_S2 "v[96:111]"
#define R_S3 "v[112:127]"
#define R_S0L "v[64:71]"
#define R_S0H "v[72:79]"
#define R_S1L "v[80:87]"
#define R_S1H "v[88:95]"
#define R_S2L "v[96:103]"
#define R_S2H "v[104:111]"
#define R_S3L "v[112:119]"
#define R_S3H "v[120:127]"
#define R_O0 "v[128:143]"
#define R_O1 "v[144:159]"
#define R_V0A "v[160:163]"
#define R_V0B "v[164:167]"
#define R_V1A "v[168:171]"
#define R_V1B "v[172:175]"
#define R_V2A "v[176:179]"
#define R_V2B "v[180:183]"
#define R_V3A "v[184:187]"
#define R_V3B "v[188:191]"
#define R_K0M "v[192:195]"
#define R_K1M "v[196:199]"
#define R_K2M "v[200:203]"
#define R_K3M "v[204:207]"
#define R_K0R "v[208:209]"
#define R_K1R "v[210:211]"
#define R_K2R "v[212:213]"
#define R_K3R "v[214:215]"
#define R_Q0M "v[216:219]"
#define R_Q1M "v[220:223]"
#define R_Q0R "v[224:225]"
#define R_Q1R "v[226:227]"
#define QK1(s, k, q) "v_mfma_f32_32x32x16_bf16 " R_S##s ", " R_K##k##M ", " R_Q##q##M ", 0\n\t"
#define QK2(s, k, q) "v_mfma_f32_32x32x8_bf16 " R_S##s ", " R_K##k##R ", " R_Q##q##R ", " R_S##s "\n\t"
#define PV1(o, v, s) "v_smfmac_f32_32x32x32_bf16 " R_O##o ", " R_V##v##A ", " R_S##s##L ", v228\n\t"
#define PV2(o, v, s) "v_smfmac_f32_32x32x32_bf16 " R_O##o ", " R_V##v##B ", " R_S##s##H ", v228\n\t"
#define PERIOD(e, sn, k, q, sp, v, o) \
    QK1(sn, k, q) E_S##e##_0 PV1(o, v, sp) E_S##e##_1 QK2(sn, k, q) E_S##e##_2 PV2(o, v, sp) E_S##e##_3
#define LDK(k) \
    "global_load_dwordx4 " R_K##k##M ", v229, s[60:61] sc1\n\t" \
    "s_mov_b32 exec_hi, 0\n\t" \
    "global_load_dwordx2 " R_K##k##R ", v231, s[60:61] sc1\n\t"
#define LDV(v) \
    "s_mov_b64 exec, %[vmask]\n\t" \
    "global_load_dwordx4 " R_V##v##A ", v230, s[60:61] sc1\n\t" \
    "global_load_dwordx4 " R_V##v##B ", v230, s[60:61] offset:640 sc1\n\t"
#define ADV "s_mov_b64 exec, -1\n\ts_add_u32 s60, s60, 5120\n\ts_addc_u32 s61, s61, 0\n\t"
#define GROUP(n, k, v) "s_waitcnt vmcnt(" n ")\n\t" LDK(k) LDV(v) ADV
#define PERIOD_NOQK(e, sp, v, o) E_S##e##_0 PV1(o, v, sp) E_S##e##_1 E_S##e##_2 PV2(o, v, sp) E_S##e##_3
constexpr u64 VMASK = 0x000fffff000fffffull;

struct Attn { f32x32 sa, sb, oo; u32x32 vv; u32x16 km; u32x8 kr, qm; u32x4 qr, misc; const char* base; };
DEV void attn_prologue1(Attn& a, const char* kv, const char* q0, const char* q1) {
    a.base = kv - 2 * 5120;
    asm volatile(
        ".irp r,208,210,212,214\n\tv_mov_b32 v\\r, 0x3f803f80\n\t.endr\n\t"
        ".irp r,209,211,213,215\n\tv_mov_b32 v\\r, 0x3f80\n\t.endr\n\t"
        ".irp r,224,225,226,227\n\tv_mov_b32 v\\r, 0\n\t.endr\n\t"
        "global_load_dwordx4 " R_Q0M ", v229, %[q0] sc1\n\t"
        "global_load_dwordx4 " R_Q1M ", v229, %[q1] sc1\n\t"
        "s_mov_b32 exec_hi, 0\n\t"
        "global_load_dwordx2 " R_Q0R ", v231, %[q0] sc1\n\t"
        "global_load_dwordx2 " R_Q1R ", v231, %[q1] sc1\n\t"
        "s_mov_b64 exec, -1\n\t"
        LDK(0) ADV LDK(1) ADV
        : "=&{v[192:207]}"(a.km), "=&{v[208:215]}"(a.kr), "=&{v[216:223]}"(a.qm), "=&{v[224:227]}"(a.qr), "+{s[60:61]}"(a.base)
        : "{v[228:231]}"(a.misc), [q0] "s"(q0 - 10240), [q1] "s"(q1 - 10240)
        : "memory", "scc");
}
DEV void attn_init_v(Attn& a, unsigned vinit) {
    asm volatile(".irp r,160,161,162,163,164,165,166,167,168,169,170,171,172,173,174,175,176,177,178,179,180,181,182,183,184,185,186,187,188,189,190,191\n\tv_mov_b32 v\\r, %[vinit]\n\t.endr"
                 : "=&{v[160:191]}"(a.vv) : [vinit] "v"(vinit));
}
DEV void attn_init_o(Attn& a) {
    asm volatile(".irp r,128,129,130,131,132,133,134,135,136,137,138,139,140,141,142,143,144,145,146,147,148,149,150,151,152,153,154,155,156,157,158,159\n\tv_mov_b32 v\\r, 0\n\t.endr"
                 : "={v[128:159]}"(a.oo));
}
DEV void attn_init_s(Attn& a) {
    asm volatile(".irp r,112,113,114,115,116,117,118,119,120,121,122,123,124,125,126,127\n\tv_mov_b32 v\\r, 0\n\t.endr" : "={v[96:127]}"(a.sb));
}
DEV void attn_prologue2(Attn& a) {
    asm volatile(LDK(2) LDV(0) ADV LDK(3) LDV(1) ADV
                 : "+{v[160:191]}"(a.vv), "+{v[192:207]}"(a.km), "+{v[208:215]}"(a.kr), "+{s[60:61]}"(a.base)
                 : "{v[228:231]}"(a.misc), [vmask] "s"(VMASK) : "memory", "scc");
}
template <int N> DEV void attn_first_scores(Attn& a) {
    asm volatile("s_waitcnt vmcnt(%[n])\n\ts_nop 1\n\t" QK1(0, 0, 0) QK1(1, 0, 1) QK2(0, 0, 0) QK2(1, 0, 1) "s_nop 11\n\t"
                 : "=&{v[64:95]}"(a.sa), "+{v[192:207]}"(a.km), "+{v[208:215]}"(a.kr), "+{v[216:223]}"(a.qm), "+{v[224:227]}"(a.qr)
                 : [n] "i"(N) : "memory");
}
#define ITER(n0, n1, n2, n3, e0, e1, e2, e3) \
        GROUP(n0, 0, 2) e0 PERIOD(0, 2, 1, 0, 3, 3, 1) PERIOD(1, 3, 1, 1, 0, 0, 0) \
        GROUP(n1, 1, 3) e1 PERIOD(2, 0, 2, 0, 1, 0, 1) PERIOD(3, 1, 2, 1, 2, 1, 0) \
        GROUP(n2, 2, 0) e2 PERIOD(0, 2, 3, 0, 3, 1, 1) PERIOD(1, 3, 3, 1, 0, 2, 0) \
        GROUP(n3, 3, 1) e3 PERIOD(2, 0, 0, 0, 1, 2, 1) PERIOD(3, 1, 0, 1, 2, 3, 0)
#define ATTN_STATE(a) "+{v[64:95]}"(a.sa), "+{v[96:127]}"(a.sb), "+{v[128:159]}"(a.oo), "+{v[160:191]}"(a.vv), "+{v[192:207]}"(a.km), "+{v[208:215]}"(a.kr), "+{s[60:61]}"(a.base)
#define ATTN_IN(a) "{v[216:223]}"(a.qm), "{v[224:227]}"(a.qr), "{v[228:231]}"(a.misc), [vmask] "s"(VMASK)
template <int X> DEV void attn_iter(Attn& a) {
    asm volatile(ITER("%[n]", "%[n]", "4", "4", , , , ) : ATTN_STATE(a) : ATTN_IN(a), [n] "i"(4 + X) : "memory", "scc");
}
#define PF1(r, p, o) "global_load_dwordx4 " r ", %[v], " p o " sc1\n\t"
#define PF2(r0, r1, p) PF1(r0, p, "") PF1(r1, p, " offset:1024")
#define PFA "s_add_u32 s62, s62, 69632\n\ts_addc_u32 s63, s63, 0\n\t"
#define PFB "s_add_u32 s64, s64, 69632\n\ts_addc_u32 s65, s65, 0\n\t"
DEV void attn_last(Attn& a, u32x32& pfa, u32x32& pfb, u32x8& pfc, const char* pa, const char* pb, unsigned voff) {
    asm volatile(
        ITER("4", "5", "6", "6", PF1("v[0:3]", "s[62:63]", ""), PF1("v[16:19]", "s[62:63]", " offset:1024") PFA, PF1("v[4:7]", "s[62:63]", ""), PF1("v[20:23]", "s[62:63]", " offset:1024") PFA)
        "s_setprio 1\n\t"
        ITER("6", "6", "6", "6", PF1("v[8:11]", "s[62:63]", ""), PF1("v[24:27]", "s[62:63]", " offset:1024") PFA, PF1("v[12:15]", "s[62:63]", ""), PF1("v[28:31]", "s[62:63]", " offset:1024"))
        "s_waitcnt vmcnt(6)\n\t" LDV(2) ADV PF2("v[32:35]", "v[48:51]", "s[64:65]") PFB
        PERIOD(0, 2, 1, 0, 3, 3, 1) LDV(3) ADV
        PF2("v[36:39]", "v[52:55]", "s[64:65]") PFB
        PERIOD(1, 3, 1, 1, 0, 0, 0)
        "s_waitcnt vmcnt(9)\n\t"
        PF2("v[40:43]", "v[56:59]", "s[64:65]") PFB
        PERIOD(2, 0, 2, 0, 1, 0, 1)
        PF2("v[44:47]", "v[60:63]", "s[64:65]") PFB
        PERIOD(3, 1, 2, 1, 2, 1, 0)
        "s_setprio 0\n\ts_waitcnt vmcnt(6)\n\t"
        PF2("v[232:235]", "v[236:239]", "s[64:65]")
        PERIOD(0, 2, 3, 0, 3, 1, 1)
        PERIOD(1, 3, 3, 1, 0, 2, 0)
        PERIOD_NOQK(2, 1, 2, 1) PERIOD_NOQK(3, 2, 3, 0)
        "s_nop 1\n\t" PV1(1, 3, 3) PV2(1, 3, 3) "s_nop 15\n\t"
        : ATTN_STATE(a), "=&{v[0:31]}"(pfa), "=&{v[32:63]}"(pfb), "=&{v[232:239]}"(pfc), "+{s[62:63]}"(pa), "+{s[64:65]}"(pb)
        : ATTN_IN(a), [v] "v"(voff)
        : "memory", "scc");
}
template <int O, class V> DEV float max16(const V& s) {
    float a = fmaxf(fmaxf(s[O], s[O + 1]), s[O + 2]), b = fmaxf(fmaxf(s[O + 3], s[O + 4]), s[O + 5]);
    a = fmaxf(fmaxf(a, s[O + 6]), s[O + 7]); b = fmaxf(fmaxf(b, s[O + 8]), s[O + 9]);
    a = fmaxf(fmaxf(a, s[O + 10]), s[O + 11]); b = fmaxf(fmaxf(b, s[O + 12]), s[O + 13]);
    a = fmaxf(fmaxf(a, s[O + 14]), s[O + 15]);
    return fmaxf(a, b);
}
DEV void attn_set_reference(Attn& a, float (&c)[2], int hh) {
#pragma unroll
    for (int i = 0; i < 2; ++i) {
        c[i] = swap_max(c[i]);
        const u32x2 t = split3(-c[i]);
        if (hh) { a.qr[2 * i] = t[0]; a.qr[2 * i + 1] = t[1]; }
    }
}

struct Ring {
    const char *x, *wq, *wgu;
    unsigned xo0, xo1, wo0, wo1, go0, go1, go2;
    unsigned lds0; int w, s0;
};
template <int C, int P> DEV void ring_piece(const Ring& r) {
    if constexpr (C < 20) {
        int cc = r.s0 + C % 10; cc = cc >= 10 ? cc - 10 : cc;
        if constexpr (C < 10) {
            const unsigned d = r.lds0 + (C & 3) * 32768 + r.w * 2048;
            if constexpr (P == 0) glds16(r.xo0, r.x + cc * 128, d);
            if constexpr (P == 1) glds16(r.xo1, r.x + cc * 128, d + 1024);
            if constexpr (P == 2) glds16(r.wo0, r.wq + cc * (64 * 3840), d + 16384);
            if constexpr (P == 3) glds16(r.wo1, r.wq + cc * (64 * 3840), d + 16384 + 1024);
        } else {
            const unsigned d = r.lds0 + (C & 3) * 32768 + r.w * 3072 + (r.w >> 2) * 32;
            const char* b = r.wgu + cc * (64 * 3072);
            if constexpr (P == 0) glds16(r.go0, b, d);
            if constexpr (P == 1) glds16(r.go1, b, d + 1024);
            if constexpr (P == 2) glds16(r.go2, b, d + 2048);
        }
    }
}
template <int C> DEV void ring_issue(const Ring& r) { ring_piece<C, 0>(r); ring_piece<C, 1>(r); ring_piece<C, 2>(r); ring_piece<C, 3>(r); }
constexpr int ring_n(int c) { return c < 10 ? 4 : (c < 20 ? 3 : 0); }
constexpr int vm_pre(int c) { return c == 6 ? 4 : (c == 15 ? 5 : 0); }
constexpr int vm_post(int c) { return (c >= 0 && c <= 4 ? 1 : 0) + ((c == 2 || c == 11) ? 1 : 0) + ((c == 9 || c == 12 || c == 16) ? 4 : 0) + (c == 17 ? 8 : 0); }
constexpr int vm_wait(int c) {
    if (c == 11) return ring_n(14);
    return vm_post(c - 3) + vm_pre(c - 2) + ring_n(c + 2) + vm_post(c - 2) + vm_pre(c - 1) + ring_n(c + 3) + vm_post(c - 1);
}

__global__ __launch_bounds__(512, 2) void fused_block(const char* x, const char* wqkv, const char* wo, const char* wg, const char* wu, const char* wd,
                                                      char* out, char* ws) {
    __shared__ __attribute__((aligned(16))) char smem[L_TOTAL];
    LAS char* lds = (LAS char*)smem;
    const int w = __builtin_amdgcn_readfirstlane(threadIdx.x >> 6);
    const int bid = blockIdx.x, xcd = bid & 7, rr = bid >> 3;
#define LANE_IDS int lane = __builtin_amdgcn_mbcnt_hi(~0u, __builtin_amdgcn_mbcnt_lo(~0u, 0u)); asm volatile("" : "+v"(lane)); \
    const int tid = 64 * w + lane, r32 = lane & 31, hh = lane >> 5; (void)tid; (void)r32; (void)hh
    asm volatile("" : "+s"(x), "+s"(wqkv), "+s"(wo), "+s"(wg), "+s"(wu), "+s"(wd), "+s"(out), "+s"(ws));
    const __amdgpu_buffer_rsrc_t wsr = __builtin_amdgcn_make_buffer_rsrc(ws, 0, WS_END, 0x00020000);
    LAS unsigned* lflag = (LAS unsigned*)(lds + L_FLAG);
    u64 tag;
    STAMP_DECL;
    STAMP(0);
    const int mi = rr & 7, ji = rr >> 3, m = 8 * (xcd & 1) + mi, j = 4 * (xcd >> 1) + ji;
    const int wm = w & 3, wn = w >> 2;
    const int g = 4 * xcd + (rr >> 3), mq = rr & 7, qq = w & 3, kh = w >> 2, odd = g & 1;
    const char* qp = ws + WS_Q + ((g >> 1) * 64 + 8 * mq + 2 * qq) * 2560;
    const unsigned kvoff = WS_KV + ((g >> 1) * 64 + 32 * kh) * 5120;
    Attn at;
    float c[2];
    {
        LANE_IDS;
        Ring rg;
        rg.x = x + m * 128 * 1280; rg.wq = wqkv + (j < 15 ? j : 14) * 256; rg.wgu = (w < 4 ? wg : wu) + j * 192;
        rg.lds0 = (unsigned)(size_t)lds; rg.w = w;
        rg.s0 = (((5 * ji + 1) >> 1) + mi + (mi >> 2)) % 10;
        rg.xo0 = (16 * w + (lane >> 3)) * 1280 + (((lane & 7) ^ (lane >> 4)) << 4);
        rg.xo1 = (16 * w + 8 + (lane >> 3)) * 1280 + (((lane & 7) ^ (4 + (lane >> 4))) << 4);
        rg.wo0 = (8 * w + (lane >> 4)) * 3840 + (((lane & 15) ^ ((lane >> 4) << 2)) << 4);
        rg.wo1 = rg.wo0 + 4 * 3840;
        ring_issue<0>(rg); __builtin_amdgcn_s_barrier();
        ring_issue<1>(rg);
        asm volatile("s_load_dwordx2 %0, %1, 0x0\n\ts_waitcnt lgkmcnt(0)" : "=s"(tag) : "s"(ws) : "memory");
        tag += 1;
        const unsigned tag32 = (unsigned)tag;
        { const int p0 = (w & 3) * 192 + lane, p1 = p0 + 64, p2 = p0 + 128;
          rg.go0 = (p0 / 12) * 3072 + (p0 % 12) * 16; rg.go1 = (p1 / 12) * 3072 + (p1 % 12) * 16; rg.go2 = (p2 / 12) * 3072 + (p2 % 12) * 16; }
        if (tid < 4) lflag[tid] = 0u;

        const int g4 = lane >> 4, q4 = (lane >> 2) & 3, p4 = lane & 3, rot = wm ^ (2 * wn);
        const int xrow = 32 * wm + r32;
        const unsigned xa = (xrow * 128 + ((hh ^ ((xrow >> 1) & 7)) << 4)) ^ (rot << 5);
        const unsigned wab = 16384 + (rot << 12) + (8 * (g4 >> 1) + q4) * 256 + (g4 & 1) * 32 + (p4 & 1) * 16 + (p4 >> 1) * 8;
        const unsigned wa0 = wab + (((2 * wn) ^ q4) << 6), wa1 = wab + (((2 * wn + 1) ^ q4) << 6);
        LAS float* part = (LAS float*)(lds + L_PART); LAS u32x2* fin = (LAS u32x2*)(lds + L_FIN);
        bf16x8 xf[40], fw[2][3];
        f32x16 acc[2] = {};
        float rs = 0.f, rq = 0.f, cs0 = 0.f, cs1 = 0.f, pv[4];
        u32x2 gr[4], fa;
        const unsigned col2 = (mi & 3) == 3 ? 0x3f803f80u : 0u;
        int pw_row = (2 * wn + hh) * 384 + xrow, pw_col = wm * 384 + 256 + 64 * wn + lane, pr_i = w < 6 ? tid : lane;
        unsigned pub_o = tid * 8 + (w < 6 ? WS_GA + bid * 3072 : OOB), warm_o = min(lane, 47) * 16;
        asm volatile("" : "+v"(pw_row), "+v"(pw_col), "+v"(pr_i), "+v"(pub_o), "+v"(warm_o), "+v"(rg.go0), "+v"(rg.go1), "+v"(rg.go2));
        const char* warm_p = rg.wgu - ji * 192 + (4 * rr + wm) * (5 * 3072);
        const unsigned gab = w < 4 ? WS_GA + (xcd + 8 * mi) * 3072 + w * 512 : WS_GA + (xcd + 64 * ji + 32 * ((w >> 1) & 1)) * 3072 + 2048 + (w & 1) * 512;
        const unsigned gao = gab + lane * 8, gas = w < 4 ? 64 * 3072 : 8 * 3072;
#define RW1(f, b, c, k) fw[f][b] = ldstr(lds + ((c) & 3) * 32768 + ((b ? wa1 : wa0) ^ ((k) << 12)), lds + ((c) & 3) * 32768 + ((b ? wa1 : wa0) ^ ((k) << 12)) + 1024)
#define RX1(c, k) xf[4 * (c) + (k)] = lds128(lds + ((c) & 3) * 32768 + (xa ^ ((k) << 5)))
#define M1(f, b, c, k) acc[b] = MFMA32(fw[f][b], xf[4 * (c) + (k)], acc[b]); SBAR()
        STAMPP(2, 1);
        wait_bar<4>();
        STAMPP(2, 2);
        RW1(0, 0, 0, 0); RX1(0, 0); RW1(0, 1, 0, 0); RW1(1, 0, 0, 1); RX1(0, 1); RW1(1, 1, 0, 1); SBAR();
        ring_issue<2>(rg); ring_piece<3, 0>(rg); SBAR();
#define A1C(c, H3, H4, H5, H6) do { constexpr int n_ = c < 9 ? c + 1 : 0; \
        M1(0, 0, c, 0); RW1(0, 0, c, 2); RX1(c, 2); ring_piece<c + 3, 1>(rg); SBAR(); \
        M1(0, 1, c, 0); RW1(0, 1, c, 2); ring_piece<c + 3, 2>(rg); SBAR(); \
        M1(1, 0, c, 1); RW1(1, 0, c, 3); RX1(c, 3); ring_piece<c + 3, 3>(rg); SBAR(); \
        M1(1, 1, c, 1); RW1(1, 1, c, 3); H3; SBAR(); \
        M1(0, 0, c, 2); H4; SBAR(); M1(0, 1, c, 2); H5; SBAR(); \
        wait_bar<vm_wait(c)>(); \
        M1(1, 0, c, 3); \
        if constexpr (c < 9) { RW1(0, 0, n_, 0); RX1(n_, 0); RW1(0, 1, n_, 0); RW1(1, 0, n_, 1); RX1(n_, 1); } \
        H6; SBAR(); \
        M1(1, 1, c, 3); \
        if constexpr (c < 9) { RW1(1, 1, n_, 1); ring_piece<c + 4, 0>(rg); } else ring_issue<c + 4>(rg); \
        SBAR(); } while (0)
#define WARM(i) glds16(warm_o, warm_p + (i) * 3072, rg.lds0 + L_TR + w * 2048)
        A1C(0, , , ({ rs = dot_ones(xf[0], rs); rq = dot_self(xf[0], rq); rs = dot_ones(xf[1], rs); rq = dot_self(xf[1], rq);
                      cs0 = dot_ones(fw[0][0], cs0); cs1 = dot_ones(fw[0][1], cs1); }), );
        STAMPP(2, 3);
        A1C(1, WARM(0), , ({ rs = dot_ones(xf[4], rs); rq = dot_self(xf[4], rq); rs = dot_ones(xf[5], rs); rq = dot_self(xf[5], rq);
                      cs0 = dot_ones(fw[0][0], cs0, col2); cs1 = dot_ones(fw[0][1], cs1, col2); }), );
        STAMPP(2, 4);
        A1C(2, WARM(1), ({ cs0 = swap_add(cs0); cs1 = swap_add(cs1); part[pw_col] = hh ? cs1 : cs0; }),
            ({ float r2 = dot_ones(xf[8], rs), q2 = dot_self(xf[8], rq); r2 = dot_ones(xf[9], r2); q2 = dot_self(xf[9], q2);
               part[pw_row] = (ji & 1) ? rs : r2; part[pw_row + 128] = (ji & 1) ? rq : q2; }),
            ({ pv[0] = part[pr_i]; pv[1] = part[384 + pr_i]; pv[2] = part[768 + pr_i]; pv[3] = part[1152 + pr_i]; }));
        STAMPP(2, 5);
        A1C(3, WARM(2), __builtin_amdgcn_raw_buffer_store_b64((u32x2){__float_as_uint((pv[0] + pv[1]) + (pv[2] + pv[3])), tag32}, wsr, pub_o, 0, SC1), , );
        STAMPP(2, 6); A1C(4, WARM(3), , , ); STAMPP(2, 7); A1C(5, WARM(4), , , ); STAMPP(2, 8);
        A1C(6, , , , ({ gr[0] = ld2_async(ws, gao); gr[1] = ld2_async(ws, gao + gas); gr[2] = ld2_async(ws, gao + 2 * gas); gr[3] = ld2_async(ws, gao + 3 * gas); }));
        STAMPP(2, 9); A1C(7, , , , ); STAMPP(2, 10);
        float rstd, st0, st1;
        u32x2 nmu, cf[3];
        A1C(8, ({ asm volatile("s_waitcnt vmcnt(6)" : "+v"(gr[0]), "+v"(gr[1]), "+v"(gr[2]), "+v"(gr[3]) :: "memory");
                  st0 = gran_sum(ws, gao, gas, gr, tag32); }),
            ({ u32x2 t = split3(st0); if (w < 4) t[0] = __float_as_uint(st0);
               fin[(w < 6 ? 64 * w : 64 * w + 64) + lane] = t; }), ,
            ({ st0 = *(const LAS float*)(fin + xrow); st1 = *(const LAS float*)(fin + 128 + xrow);
               cf[0] = fin[256 + 192 * hh + 64 * wn + r32]; cf[1] = fin[256 + 192 * hh + 64 * wn + 32 + r32]; }));
        A1C(9, ({ const float mean = st0 * (1.f / 640.f), var = fmaxf(st1 * (1.f / 640.f) - mean * mean, 0.f);
                  nmu = (u32x2){pk2(-mean, -mean), pk2(-mean, 0.f)};
                  rstd = __builtin_amdgcn_rcpf(bfr(__builtin_amdgcn_sqrtf(bfr(bfr(var) + 1e-5f))));
                  asm volatile("" : "+v"(nmu), "+v"(rstd)); }),
            , ({ acc[0] = MFMA8(cf[0], nmu, acc[0]); acc[1] = MFMA8(cf[1], nmu, acc[1]); }), );
        STAMPP(2, 11);
        STAMPP(1, 1);
        { const int tile = 4 * m + wm;
          const float sc = j < 5 ? rstd * QSCALE : rstd;
          u32x4 y[4];
#pragma unroll
          for (int i = 0; i < 4; ++i)
#pragma unroll
              for (int e = 0; e < 4; ++e) y[i][e] = pk2(sc * acc[i >> 1][8 * (i & 1) + 2 * e], sc * acc[i >> 1][8 * (i & 1) + 2 * e + 1]);
          if (j < 10) {
              const unsigned base = (j < 5 ? WS_Q + tile * 2560 : WS_KV + tile * 5120) + r32 * 16, ps = j < 5 ? 64 * 2560 : 64 * 5120;
              const int c8 = (j < 5 ? j : j - 5) * 16 + 8 * wn + hh;
#pragma unroll
              for (int i = 0; i < 4; ++i) {
                  const int ci = c8 + 2 * i, pr = ci / 5, pl = ci - 5 * pr;
                  __builtin_amdgcn_raw_buffer_store_b128(y[i], wsr, base + pr * ps + pl * 512, 0, SC1);
              }
          } else {
              LAS char* tp = lds + L_TR + w * 2048;
              const int cu = 2 * (g4 & 1) + (p4 >> 1);
#pragma unroll
              for (int b = 0; b < 2; ++b) {
#pragma unroll
                  for (int v = 0; v < 2; ++v) *(LAS u32x4*)(tp + r32 * 64 + (((2 * v + hh) ^ ((r32 >> 1) & 3)) << 4)) = y[2 * b + v];
                  asm volatile("s_waitcnt lgkmcnt(0)" ::: "memory");
                  const int col = (j - 10) * 128 + 64 * wn + 32 * b + r32, head = col / 20, d = col - 20 * head;
                  const unsigned base = j < 15 ? WS_KV + ((head >> 1) * 64 + tile) * 5120 + 2560 + (head & 1) * 1280 + d * 16 + hh * 320 : OOB;
#pragma unroll
                  for (int i = 0; i < 2; ++i) {
                      const int r0 = 16 * i + 8 * hh + q4;
                      const bf16x8 v = ldstr(tp + r0 * 64 + ((cu ^ (q4 >> 1)) << 4) + (p4 & 1) * 8,
                                             tp + (r0 + 4) * 64 + ((cu ^ (2 + (q4 >> 1))) << 4) + (p4 & 1) * 8);
                      __builtin_amdgcn_raw_buffer_store_b128(__builtin_bit_cast(u32x4, v), wsr, base + i * 640, 0, SC1);
                  }
                  asm volatile("s_waitcnt lgkmcnt(0)" ::: "memory");
              }
          } }
        STAMPP(1, 2);
        unsigned ga[4];
#pragma unroll
        for (int ks = 0; ks < 4; ++ks) ga[ks] = (g4 & 1) * (12288 + 32) + (8 * (g4 >> 1) + q4) * 192 + wn * 96 + p4 * 8 + (ks ^ rot) * 3072;
        float cs[3] = {0.f, 0.f, 0.f};
        f32x16 ac2[3] = {};
#define RW2(f, b, c, k) fw[f][b] = ldstr(lds + ((c) & 3) * 32768 + ga[k] + b * 32, lds + ((c) & 3) * 32768 + ga[k] + b * 32 + 768)
#define M2(f, b, c, k) ac2[b] = MFMA32(fw[f][b], xf[4 * ((c) - 10) + (k)], ac2[b]); SBAR()
        SBAR(); RW2(0, 0, 10, 0); RW2(0, 1, 10, 0); RW2(0, 2, 10, 0); RW2(1, 0, 10, 1); RW2(1, 1, 10, 1); RW2(1, 2, 10, 1); SBAR();
#define A2C(c, H0, H1, H2, H3, H5, H6) do { \
        M2(0, 0, c, 0); RW2(0, 0, c, 2); H0; SBAR(); \
        M2(0, 1, c, 0); RW2(0, 1, c, 2); H1; SBAR(); \
        M2(0, 2, c, 0); RW2(0, 2, c, 2); H2; SBAR(); \
        M2(1, 0, c, 1); RW2(1, 0, c, 3); H3; SBAR(); \
        M2(1, 1, c, 1); RW2(1, 1, c, 3); SBAR(); \
        M2(1, 2, c, 1); RW2(1, 2, c, 3); SBAR(); \
        M2(0, 0, c, 2); M2(0, 1, c, 2); H5; SBAR(); \
        if constexpr (c < 19) wait_bar<vm_wait(c)>(); \
        M2(0, 2, c, 2); \
        if constexpr (c < 19) { RW2(0, 0, c + 1, 0); RW2(0, 1, c + 1, 0); RW2(0, 2, c + 1, 0); } \
        H6; SBAR(); \
        M2(1, 0, c, 3); if constexpr (c < 19) RW2(1, 0, c + 1, 1); ring_piece<c + 4, 0>(rg); SBAR(); \
        M2(1, 1, c, 3); if constexpr (c < 19) RW2(1, 1, c + 1, 1); ring_piece<c + 4, 1>(rg); SBAR(); \
        M2(1, 2, c, 3); if constexpr (c < 19) RW2(1, 2, c + 1, 1); ring_piece<c + 4, 2>(rg); SBAR(); } while (0)
        A2C(10, , , , , ({ cs[0] = dot_ones(fw[0][0], cs[0]); cs[1] = dot_ones(fw[0][1], cs[1]); cs[2] = dot_ones(fw[0][2], cs[2]); }), );
        A2C(11, , , , , ({ cs[0] = dot_ones(fw[0][0], cs[0], col2); cs[1] = dot_ones(fw[0][1], cs[1], col2); cs[2] = dot_ones(fw[0][2], cs[2], col2);
                           cs[0] = swap_add(cs[0]); cs[1] = swap_add(cs[1]); cs[2] = swap_add(cs[2]);
                           part[wm * 384 + 96 * wn + lane] = hh ? cs[1] : cs[0]; part[wm * 384 + 96 * wn + 64 + r32] = cs[2]; }),
            ({ const int pi = w < 3 ? tid : lane; pv[0] = part[pi]; pv[1] = part[384 + pi]; pv[2] = part[768 + pi]; pv[3] = part[1152 + pi]; }));
        if (tid == 0) flag_store(ws + WS_FA + bid * 8, tag);
        A2C(12, __builtin_amdgcn_raw_buffer_store_b64((u32x2){__float_as_uint((pv[0] + pv[1]) + (pv[2] + pv[3])), tag32}, wsr, tid * 8 + (w < 3 ? WS_GB + bid * 1536 : OOB), 0, SC1), , , , , );
        const int unit = bid + 256 * w, us = unit / 10, nb = unit - 10 * us;
        const char* psrc = us < 48 ? wd : wo;
        const u32x4 psrd = {(unsigned)(size_t)psrc, (unsigned)((size_t)psrc >> 32) & 0xffffu, unit < 680 ? (us < 48 ? 1536u * 1280u : 640u * 1280u) : 0u, 0x00020000u};
        const unsigned plds = L_TR + min(w, 3) * 4096, pvo = (hh * (us < 64 ? 8 : 20) + ((lane >> 3) & 3)) * 1280 + (lane & 7) * 16;
#define PLD(i) ({ const int kap = 4 * us + i, r0 = us < 48 ? 16 * (kap >> 1) + 4 * (kap & 1) : (us < 64 ? 20 * ((kap >> 1) - 96) + 4 * (kap & 1) : 40 * (kap - 256) + 16); \
            asm volatile("s_mov_b32 m0, %3\n\ts_nop 0\n\tbuffer_load_dwordx4 %0, %1, %2 offen lds" :: "v"(pvo), "s"(psrd), "s"(r0 * 1280 + nb * 128), "s"(rg.lds0 + plds + i * 1024) : "memory"); })
        A2C(13, PLD(0), PLD(1), PLD(2), PLD(3), , );
        A2C(14, , , , , , );
        const int gbh = w >= 3 && w < 6;
        const unsigned gbo = WS_GB + (xcd + 64 * ji + 32 * gbh) * 1536 + (w < 6 ? w - 3 * gbh : 0) * 512 + lane * 8, fao = WS_FA + (tid & 255) * 8;
        A2C(15, , , , , , ({ gr[0] = ld2_async(ws, gbo); gr[1] = ld2_async(ws, gbo + 8 * 1536); gr[2] = ld2_async(ws, gbo + 16 * 1536); gr[3] = ld2_async(ws, gbo + 24 * 1536);
                   fa = ld2_async(ws, fao); }));
        A2C(16, , , , , , );
        const unsigned pdst = lane * 16 + (unit < 680 ? WS_WP + (4 * nb * 68 + us) * 1024 : OOB);
        const LAS char* prd = lds + plds + (8 * g4 + q4) * 128 + p4 * 8;
        bf16x8 pk[4];
#define PST(i) __builtin_amdgcn_raw_buffer_store_b128(__builtin_bit_cast(u32x4, pk[i]), wsr, pdst + i * (68 * 1024), 0, SC1)
        A2C(17, ({ asm volatile("s_waitcnt vmcnt(3)" : "+v"(gr[0]), "+v"(gr[1]), "+v"(gr[2]), "+v"(gr[3]), "+v"(fa) :: "memory");
                   st0 = gran_sum(ws, gbo, 8 * 1536, gr, tag32);
                   flag_wait(ws, fao, fa, tag); }),
            ({ fin[256 + (w < 6 ? tid : lane)] = split3(st0);
               pk[0] = ldstr(prd, prd + 512); pk[1] = ldstr(prd + 32, prd + 544); }),
            ({ pk[2] = ldstr(prd + 64, prd + 576); pk[3] = ldstr(prd + 96, prd + 608); }), ,
            ({ PST(0); PST(1); PST(2); PST(3); }),
            ({ cf[0] = fin[256 + 192 * hh + 96 * wn + r32]; cf[1] = fin[256 + 192 * hh + 96 * wn + 32 + r32]; cf[2] = fin[256 + 192 * hh + 96 * wn + 64 + r32]; }));
        A2C(18, at.misc = ((u32x4){0xddddu, 10240u + odd * 1536 + lane * 16, 2560u + odd * 1280 + hh * 320 + r32 * 16, 10240u + 1024 + r32 * 16 + odd * 8}),
            attn_prologue1(at, ws + kvoff, qp + kh * 2560, qp + (kh ^ 1) * 2560), ,
            ({ ac2[0] = MFMA8(cf[0], nmu, ac2[0]); ac2[1] = MFMA8(cf[1], nmu, ac2[1]); ac2[2] = MFMA8(cf[2], nmu, ac2[2]); }), , );
        A2C(19, attn_init_v(at, r32 == 20 ? 0x3f803f80u : 0u),
            attn_prologue2(at), attn_init_o(at), attn_init_s(at), , );
        STAMPP(1, 3);
        __builtin_amdgcn_s_setprio(3);
        attn_first_scores<10>(at);
        { const unsigned base = WS_HP + (8 * m + 2 * wm + (r32 >> 4)) * (272 * 256) + (2 * (6 * j + 3 * wn) + hh) * 256 + (r32 & 15) * 16;
          const float r2 = rstd * rstd;
#pragma unroll
          for (int b = 0; b < 3; ++b) {
              u32x4 v;
#pragma unroll
              for (int e = 0; e < 4; ++e) v[e] = pk2(ac2[b][2 * e] * r2 * ac2[b][8 + 2 * e], ac2[b][2 * e + 1] * r2 * ac2[b][9 + 2 * e]);
              __builtin_amdgcn_raw_buffer_store_b128(v, wsr, base + b * 512, 0, SC1);
          } }
        STAMPP(1, 4); STAMPP(3, 1);
        c[0] = max16<0>(at.sa); c[1] = max16<16>(at.sa);
    }
    const int rb = 4 * xcd + (rr >> 3), ct = rr & 7;
    const unsigned aob = WS_HP + 4 * rb * (272 * 256), bob = WS_WP + 5 * ct * (68 * 1024);
    const int krot = 2 * (((rr >> 3) + ct) % 3), k2 = 6 * w + (krot + 2) % 6, k4 = 6 * w + (krot + 4) % 6;
    u32x32 pfa, pfb; u32x8 pfc;
#pragma unroll 1
    for (int attempt = 0;; ++attempt) {
        LANE_IDS;
        attn_set_reference(at, c, hh);
        asm volatile("" : "+v"(c[0]), "+v"(c[1]));
        attn_first_scores<63>(at);
        at.base = ws + kvoff + 2 * 5120;
        STAMPP(1, 5); STAMPP(3, 2);
        attn_iter<3>(at); STAMPP(3, 3);
        arrive_publish(lflag + 1, lane, ws + WS_FH + bid * 8, tag);
        __builtin_amdgcn_s_setprio(2);
        attn_iter<0>(at); STAMPP(3, 4);
        attn_iter<0>(at); STAMPP(3, 5);
        u32x4 fh0 = ld4_async(ws, WS_FH + lane * 16), fh1 = ld4_async(ws, WS_FH + 1024 + lane * 16);
        attn_iter<2>(at); STAMPP(3, 6);
        asm volatile("" : "+v"(fh0), "+v"(fh1));
        flag_wait4(ws, WS_FH + lane * 16, fh0, fh1, tag);
        if (bid == 0 && tid == 0) flag_store(ws + WS_EPOCH, tag);
        { const unsigned li = (rr * 8 + w) * 25 + min(lane, 24), lt = li / 160;
          glds4(WS_WP + lt * (68 * 1024) + 48 * 1024 + (li - 160 * lt) * 128, ws, (unsigned)(size_t)lds + L_TR + w * 256); }
        STAMPP(3, 7);
        attn_iter<1>(at); STAMPP(3, 8); STAMPP(3, 9);
        attn_last(at, pfa, pfb, pfc, ws + aob + (6 * w + krot) * 1024, ws + bob + (6 * w + krot) * 1024, lane * 16);
        STAMPP(1, 6); STAMPP(3, 10);
        { int ln = __builtin_amdgcn_mbcnt_hi(~0u, __builtin_amdgcn_mbcnt_lo(~0u, 0u)); asm volatile("" : "+v"(ln));
          LAS float* snd = (LAS float*)lds + w * 13 * 64 + ln;
          float chk0 = (at.oo[8] * 0x1p30f) * 0.f, chk1 = (at.oo[24] * 0x1p30f) * 0.f;
#pragma unroll
          for (int i = 0; i < 12; ++i) { chk0 = __builtin_fmaf(at.oo[i], 0.f, chk0); chk1 = __builtin_fmaf(at.oo[16 + i], 0.f, chk1); }
          if (!(chk0 + chk1 == 0.f)) lflag[0] = 1u;
#pragma unroll
          for (int i = 0; i < 12; ++i) snd[i * 64] = at.oo[16 + i];
          snd[12 * 64] = c[1]; }
        lds_bar();
        const unsigned redo = lflag[0];
        { int lane = __builtin_amdgcn_mbcnt_hi(~0u, __builtin_amdgcn_mbcnt_lo(~0u, 0u)); asm volatile("" : "+v"(lane));
          const int r32 = lane & 31, hh = lane >> 5;
          float f[12];
          const LAS float* rcv = (const LAS float*)lds + (w ^ 4) * 13 * 64 + lane;
          const float co = rcv[12 * 64], cx = fmaxf(c[0], co) + 1.f;
          const float wa = __builtin_amdgcn_exp2f(c[0] - cx), wb = __builtin_amdgcn_exp2f(co - cx);
#pragma unroll
          for (int i = 0; i < 12; ++i) f[i] = at.oo[i] * wa + rcv[i * 64] * wb;
          const float inv = __builtin_amdgcn_rcpf(swap_add(hh ? f[8] : 0.f));
          const unsigned base = WS_HP + (16 * mq + 4 * qq + 2 * kh + (r32 >> 4)) * (272 * 256) + (r32 & 15) * 16;
          const u32x4 v = {pk2(f[0] * inv, f[1] * inv), pk2(f[2] * inv, f[3] * inv), pk2(f[4] * inv, f[5] * inv), pk2(f[6] * inv, f[7] * inv)};
          __builtin_amdgcn_raw_buffer_store_b128(v, wsr, base + (192 + 2 * g + hh) * 256, 0, SC1);
          const u32x2 v2 = {pk2(f[8] * inv, f[9] * inv), pk2(f[10] * inv, f[11] * inv)};
          __builtin_amdgcn_raw_buffer_store_b64(v2, wsr, hh ? OOB : base + (256 + (g >> 1)) * 256 + (g & 1) * 8, 0, SC1); }
        if (attempt || __builtin_amdgcn_readfirstlane(redo) == 0u) break;
        lds_bar();
        asm volatile("s_waitcnt vmcnt(0)" :: "{v[0:31]}"(pfa), "{v[32:63]}"(pfb), "{v[232:239]}"(pfc) : "memory");
        { int ln = __builtin_amdgcn_mbcnt_hi(~0u, __builtin_amdgcn_mbcnt_lo(~0u, 0u)); asm volatile("" : "+v"(ln));
          attn_prologue1(at, ws + kvoff, qp + kh * 2560, qp + (kh ^ 1) * 2560); attn_init_v(at, (ln & 31) == 20 ? 0x3f803f80u : 0u);
          attn_prologue2(at); attn_init_o(at); attn_init_s(at); }
        attn_first_scores<0>(at);
        c[0] = -INFINITY; c[1] = -INFINITY;
#pragma unroll 1
        for (int t = 0; t < 32; ++t) {
            const u32x4 km = __builtin_amdgcn_raw_buffer_load_b128(wsr, at.misc[1] - 10240, kvoff + t * 5120, SC1);
            const u32x2 kr = __builtin_amdgcn_raw_buffer_load_b64(wsr, at.misc[3] - 10240, kvoff + t * 5120, SC1);
#pragma unroll
            for (int i = 0; i < 2; ++i) {
                const u32x4 qm = {at.qm[4 * i], at.qm[4 * i + 1], at.qm[4 * i + 2], at.qm[4 * i + 3]};
                const u32x2 qr = {at.qr[2 * i], at.qr[2 * i + 1]};
                f32x16 s = MFMA32(__builtin_bit_cast(bf16x8, km), __builtin_bit_cast(bf16x8, qm), (f32x16){});
                s = MFMA8(kr, qr, s);
                c[i] = fmaxf(c[i], max16<0>(s));
            }
        }
    }
    LANE_IDS;
    auto loada = [&](int s, u32x4 (&a)[4]) {
#pragma unroll
        for (int i = 0; i < 4; ++i) a[i] = __builtin_amdgcn_raw_buffer_load_b128(wsr, lane * 16, aob + s * 1024 + i * (272 * 256), SC1);
    };
    auto loadb = [&](int s, u32x4 (&b)[5]) {
#pragma unroll
        for (int i = 0; i < 5; ++i) b[i] = __builtin_amdgcn_raw_buffer_load_b128(wsr, lane * 16, bob + s * 1024 + i * (68 * 1024), SC1);
    };
    STAMPP(1, 7); STAMPP(3, 11);

    f32x4 acc[4][5] = {};
    auto mma = [&](const u32x4 (&a)[4], const u32x4 (&b)[5]) {
#pragma unroll
        for (int i = 0; i < 4; ++i)
#pragma unroll
            for (int n = 0; n < 5; ++n) acc[i][n] = MFMA16(__builtin_bit_cast(bf16x8, b[n]), __builtin_bit_cast(bf16x8, a[i]), acc[i][n]);
    };
    u32x4 a0[4], b0[5], a1[4], b1[5], a2[4], b2[5], a3[4], b3[5];
    SBAR();
    asm volatile("s_waitcnt vmcnt(2)" : "+{v[0:31]}"(pfa), "+{v[32:63]}"(pfb), "+{v[232:239]}"(pfc) :: "memory");
#pragma unroll
    for (int i = 0; i < 4; ++i) {
        a0[i] = (u32x4){pfa[4 * i], pfa[4 * i + 1], pfa[4 * i + 2], pfa[4 * i + 3]}; a1[i] = (u32x4){pfa[16 + 4 * i], pfa[17 + 4 * i], pfa[18 + 4 * i], pfa[19 + 4 * i]};
        b0[i] = (u32x4){pfb[4 * i], pfb[4 * i + 1], pfb[4 * i + 2], pfb[4 * i + 3]}; b1[i] = (u32x4){pfb[16 + 4 * i], pfb[17 + 4 * i], pfb[18 + 4 * i], pfb[19 + 4 * i]};
    }
    b0[4] = (u32x4){pfc[0], pfc[1], pfc[2], pfc[3]}; b1[4] = (u32x4){pfc[4], pfc[5], pfc[6], pfc[7]};
    mma(a0, b0); SBAR();
    asm volatile("s_waitcnt vmcnt(0)" ::: "memory");
    arrive_publish(lflag + 2, lane, ws + WS_FO + (mq * 32 + g) * 128, tag);
    SBAR();
    loada(k2, a2); loadb(k2, b2); loada(k2 + 1, a3); loadb(k2 + 1, b3); loada(k4, a0); loadb(k4, b0); SBAR();
    mma(a1, b1); SBAR();
    loada(k4 + 1, a1); loadb(k4 + 1, b1); SBAR();
    mma(a2, b2); SBAR();
    loadb(48 + w, b2); SBAR();
    mma(a3, b3); SBAR();
    loadb(56 + w, b3); SBAR();
    mma(a0, b0); SBAR();
    loadb(64 + (w & 3), b0);
    u32x2 xv[3]; int xoff[3];
#pragma unroll
    for (int it = 0; it < 3; ++it) {
        const int tl = min(w + 8 * it, 19), i = tl / 5, n = tl - 5 * i;
        xoff[it] = (64 * rb + 16 * i + (lane & 15)) * 1280 + (80 * ct + 16 * n + 4 * (lane >> 4)) * 2;
        xv[it] = *(const GAS u32x2*)(x + xoff[it]);
    }
    SBAR();
    mma(a1, b1); SBAR();
    STAMPP(1, 8);
    flag_wait(ws, WS_FO + ((rb >> 2) * 32 + r32) * 128, (u32x2){~(unsigned)tag, 0u}, tag);
    STAMPP(1, 9);
    loada(48 + w, a2); loada(56 + w, a3); loada(64 + (w & 3), a0); SBAR();
    mma(a2, b2); mma(a3, b3);
    if (w < 4) mma(a0, b0);
    STAMPP(1, 10);
    lds_bar();
    LAS f32x4* red = (LAS f32x4*)lds;
#pragma unroll
    for (int i = 0; i < 4; ++i)
#pragma unroll
        for (int n = 0; n < 5; ++n) red[w * 1280 + (i * 5 + n) * 64 + lane] = acc[i][n];
    lds_bar();
#pragma unroll
    for (int it = 0; it < 3; ++it) {
        if (it < 2 || w < 4) {
            const int item = tid + 512 * it;
            f32x4 s = red[item];
#pragma unroll
            for (int k = 1; k < 8; ++k) s += red[k * 1280 + item];
            u32x2 v;
            v[0] = pk2(s[0] + __uint_as_float(xv[it][0] << 16), s[1] + __uint_as_float(xv[it][0] & 0xffff0000u));
            v[1] = pk2(s[2] + __uint_as_float(xv[it][1] << 16), s[3] + __uint_as_float(xv[it][1] & 0xffff0000u));
            *(GAS u32x2*)(out + xoff[it]) = v;
        }
    }
    STAMPP(1, 11);
    STAMP_OUT;
}

extern "C" void kernel_launch(void* const* d_in, const int* in_sizes, int n_in, void* d_out, int out_size, void* d_ws, size_t ws_size,
                              hipStream_t stream) {
    fused_block<<<256, 512, 0, stream>>>((const char*)d_in[0], (const char*)d_in[1], (const char*)d_in[2], (const char*)d_in[4],
                                         (const char*)d_in[5], (const char*)d_in[6], (char*)d_out, (char*)d_ws);
}
```

```cpp
#define STAMPS 1
#define DIAG_A 0
#define DIAG_B 0
#include <hip/hip_runtime.h>
#include <hip/hip_bf16.h>

typedef __bf16 bf16x8 __attribute__((ext_vector_type(8)));
typedef __bf16 bf16x2 __attribute__((ext_vector_type(2)));
typedef short s16x4 __attribute__((ext_vector_type(4)));
typedef float f32x32 __attribute__((ext_vector_type(32)));
typedef float f32x16 __attribute__((ext_vector_type(16)));
typedef float f32x4 __attribute__((ext_vector_type(4)));
typedef unsigned u32x32 __attribute__((ext_vector_type(32)));
typedef unsigned u32x16 __attribute__((ext_vector_type(16)));
typedef unsigned u32x8 __attribute__((ext_vector_type(8)));
typedef unsigned u32x4 __attribute__((ext_vector_type(4)));
typedef unsigned u32x2 __attribute__((ext_vector_type(2)));
typedef unsigned long long u64;
#define LAS __attribute__((address_space(3)))
#define GAS __attribute__((address_space(1)))
#define DEV __device__ __forceinline__

constexpr unsigned WS_EPOCH = 0;
constexpr unsigned WS_FA = 4096;
constexpr unsigned WS_FH = 8192;
constexpr unsigned WS_FO = 16384;
constexpr unsigned WS_GA = 65536;
constexpr unsigned WS_GB = WS_GA + 256 * 384 * 8;
constexpr unsigned WS_Q = WS_GB + 256 * 192 * 8;
constexpr unsigned WS_KV = WS_Q + 16 * 64 * 2560;
constexpr unsigned WS_HP = WS_KV + 16 * 64 * 5120;
constexpr unsigned WS_WP = WS_HP + 128 * 272 * 256;
constexpr unsigned WS_END = WS_WP + 40 * 68 * 1024;
constexpr unsigned WS_STAMP = 32u << 20;
constexpr unsigned OOB = 0x40000000u;

constexpr int L_TR = 131072;
constexpr int L_PART = L_TR + 16384;
constexpr int L_FIN = L_PART + 6144;
constexpr int L_FLAG = L_FIN + 5120;
constexpr int L_TOTAL = 163840;

constexpr float QSCALE = 0.22360679774997896f * 1.4426950408889634f;
constexpr int SC1 = 16;
constexpr unsigned SPIN_CAP = 1u << 22;

DEV unsigned pk2(float lo, float hi) { bf16x2 v = {(__bf16)lo, (__bf16)hi}; return __builtin_bit_cast(unsigned, v); }
DEV float bfr(float x) { return (float)(__bf16)x; }
DEV u32x2 split3(float v) {
    const float a = bfr(v), b = bfr(v - a), c = v - a - b;
    return (u32x2){pk2(a, b), pk2(c, 0.f)};
}
DEV float swap_add(float v) {
    auto r = __builtin_amdgcn_permlane32_swap(__float_as_uint(v), __float_as_uint(v), false, false);
    return __uint_as_float(r[0]) + __uint_as_float(r[1]);
}
DEV float swap_max(float v) {
    auto r = __builtin_amdgcn_permlane32_swap(__float_as_uint(v), __float_as_uint(v), false, false);
    return fmaxf(__uint_as_float(r[0]), __uint_as_float(r[1]));
}
DEV void glds16(unsigned voff, const void* sbase, unsigned ldsdst) {
    asm volatile("s_mov_b32 m0, %2\n\ts_nop 0\n\tglobal_load_lds_dwordx4 %0, %1" ::"v"(voff), "s"(sbase), "s"(ldsdst) : "memory");
}
DEV void glds4(unsigned voff, const void* sbase, unsigned ldsdst) {
    asm volatile("s_mov_b32 m0, %2\n\ts_nop 0\n\tglobal_load_lds_dword %0, %1" ::"v"(voff), "s"(sbase), "s"(ldsdst) : "memory");
}
template <int N> DEV void wait_bar() { asm volatile("s_waitcnt vmcnt(%0) lgkmcnt(0)\n\ts_barrier" ::"i"(N) : "memory"); }
DEV void lds_bar() { asm volatile("s_waitcnt lgkmcnt(0)\n\ts_barrier" ::: "memory"); }
DEV bf16x8 lds128(const LAS char* p) { return *(const LAS bf16x8*)p; }
DEV bf16x8 ldstr(const LAS char* p0, const LAS char* p1) {
    s16x4 a = __builtin_amdgcn_ds_read_tr16_b64_v4i16((LAS s16x4*)p0), b = __builtin_amdgcn_ds_read_tr16_b64_v4i16((LAS s16x4*)p1);
    return __builtin_bit_cast(bf16x8, __builtin_shufflevector(a, b, 0, 1, 2, 3, 4, 5, 6, 7));
}
DEV float dot_ones(bf16x8 v, float acc, unsigned ones = 0x3f803f80u) {
    const bf16x2 one = __builtin_bit_cast(bf16x2, ones);
#pragma unroll
    for (int i = 0; i < 4; ++i) acc = __builtin_amdgcn_fdot2_f32_bf16((bf16x2){v[2 * i], v[2 * i + 1]}, one, acc, false);
    return acc;
}
DEV float dot_self(bf16x8 v, float acc) {
#pragma unroll
    for (int i = 0; i < 4; ++i) { bf16x2 p = {v[2 * i], v[2 * i + 1]}; acc = __builtin_amdgcn_fdot2_f32_bf16(p, p, acc, false); }
    return acc;
}
#define MFMA32(a, b, c) __builtin_amdgcn_mfma_f32_32x32x16_bf16(a, b, c, 0, 0, 0)
#define MFMA8(a, b, c) __builtin_amdgcn_mfma_f32_32x32x8bf16_1k(__builtin_bit_cast(s16x4, a), __builtin_bit_cast(s16x4, b), c, 0, 0, 0)
#define MFMA16(a, b, c) __builtin_amdgcn_mfma_f32_16x16x32_bf16(a, b, c, 0, 0, 0)
#define SBAR() __builtin_amdgcn_sched_barrier(0)

#define STAMP(i) do { u64 a_, b_; SBAR(); asm volatile("s_memrealtime %0\n\ts_memtime %1\n\ts_waitcnt lgkmcnt(0)" : "=&s"(a_), "=&s"(b_) :: "memory"); \
    st_rt[i] = (unsigned)a_; st_cy[i] = (unsigned)b_; SBAR(); } while (0)
#define STAMPP(p, i) do { if (STAMPS == p) STAMP(i); } while (0)
#define STAMP_DECL unsigned st_rt[12] = {}, st_cy[12] = {}
#define STAMP_OUT do { const unsigned d_ = st_rt[DIAG_B] - st_rt[DIAG_A]; const unsigned t_ = (unsigned)__builtin_amdgcn_s_memrealtime(); \
    while ((unsigned)__builtin_amdgcn_s_memrealtime() - t_ < d_) __builtin_amdgcn_s_sleep(1); } while (0)

DEV u32x2 ld2_async(const char* base, unsigned off) { u32x2 v; asm volatile("global_load_dwordx2 %0, %1, %2 sc1" : "=v"(v) : "v"(off), "s"(base) : "memory"); return v; }
DEV u32x4 ld4_async(const char* base, unsigned off) { u32x4 v; asm volatile("global_load_dwordx4 %0, %1, %2 sc1" : "=v"(v) : "v"(off), "s"(base) : "memory"); return v; }
DEV void flag_store(char* p, u64 tag) { __hip_atomic_store((GAS u64*)p, tag, __ATOMIC_RELAXED, __HIP_MEMORY_SCOPE_AGENT); }
DEV float gran_sum(const char* ws, unsigned off, unsigned stride, u32x2 (&g)[4], unsigned tag) {
#pragma unroll 1
    for (unsigned spin = 0; spin < SPIN_CAP; ++spin) {
        if (__all(g[0][1] == tag && g[1][1] == tag && g[2][1] == tag && g[3][1] == tag)) break;
        asm volatile("s_sleep 1\n\tglobal_load_dwordx2 %0, %4, %8 sc1\n\tglobal_load_dwordx2 %1, %5, %8 sc1\n\t"
                     "global_load_dwordx2 %2, %6, %8 sc1\n\tglobal_load_dwordx2 %3, %7, %8 sc1\n\ts_waitcnt vmcnt(0)"
                     : "=&v"(g[0]), "=&v"(g[1]), "=&v"(g[2]), "=&v"(g[3])
                     : "v"(off), "v"(off + stride), "v"(off + 2 * stride), "v"(off + 3 * stride), "s"(ws) : "memory");
    }
    return (__uint_as_float(g[0][0]) + __uint_as_float(g[1][0])) + (__uint_as_float(g[2][0]) + __uint_as_float(g[3][0]));
}
DEV void flag_wait(const char* ws, unsigned off, u32x2 f, u64 tag) {
#pragma unroll 1
    for (unsigned spin = 0; spin < SPIN_CAP; ++spin) {
        if (__all(f[0] == (unsigned)tag && f[1] == (unsigned)(tag >> 32))) break;
        asm volatile("s_sleep 1\n\tglobal_load_dwordx2 %0, %1, %2 sc1\n\ts_waitcnt vmcnt(0)" : "=&v"(f) : "v"(off), "s"(ws) : "memory");
    }
}
DEV void flag_wait4(const char* ws, unsigned off, u32x4 f, u32x4 g, u64 tag) {
    const unsigned lo = (unsigned)tag, hi = (unsigned)(tag >> 32);
#pragma unroll 1
    for (unsigned spin = 0; spin < SPIN_CAP; ++spin) {
        if (__all(f[0] == lo && f[1] == hi && f[2] == lo && f[3] == hi && g[0] == lo && g[1] == hi && g[2] == lo && g[3] == hi)) break;
        asm volatile("s_sleep 1\n\tglobal_load_dwordx4 %0, %2, %3 sc1\n\tglobal_load_dwordx4 %1, %2, %3 offset:1024 sc1\n\ts_waitcnt vmcnt(0)"
                     : "=&v"(f), "=&v"(g) : "v"(off), "s"(ws) : "memory");
    }
}
DEV void arrive_publish(LAS unsigned* cnt, int lane, char* flag, u64 tag) {
    if (lane == 0) {
        const unsigned old = __hip_atomic_fetch_add(cnt, 1u, __ATOMIC_RELAXED, __HIP_MEMORY_SCOPE_WORKGROUP);
        if (old == 7u) flag_store(flag, tag);
    }
}

#define EXP4(a, b, c, d) "v_exp_f32 v" #a ", v" #a "\n\tv_exp_f32 v" #b ", v" #b "\n\tv_exp_f32 v" #c ", v" #c "\n\tv_exp_f32 v" #d ", v" #d "\n\t"
#define E_S0_0 EXP4(64, 65, 66, 67)
#define E_S0_1 EXP4(68, 69, 70, 71)
#define E_S0_2 EXP4(72, 73, 74, 75)
#define E_S0_3 EXP4(76, 77, 78, 79)
#define E_S1_0 EXP4(80, 81, 82, 83)
#define E_S1_1 EXP4(84, 85, 86, 87)
#define E_S1_2 EXP4(88, 89, 90, 91)
#define E_S1_3 EXP4(92, 93, 94, 95)
#define E_S2_0 EXP4(96, 97, 98, 99)
#define E_S2_1 EXP4(100, 101, 102, 103)
#define E_S2_2 EXP4(104, 105, 106, 107)
#define E_S2_3 EXP4(108, 109, 110, 111)
#define E_S3_0 EXP4(112, 113, 114, 115)
#define E_S3_1 EXP4(116, 117, 118, 119)
#define E_S3_2 EXP4(120, 121, 122, 123)
#define E_S3_3 EXP4(124, 125, 126, 127)
#define R_S0 "v[64:79]"
#define R_S1 "v[80:95]"
#define R_S2 "v[96:111]"
#define R_S3 "v[112:127]"
#define R_S0L "v[64:71]"
#define R_S0H "v[72:79]"
#define R_S1L "v[80:87]"
#define R_S1H "v[88:95]"
#define R_S2L "v[96:103]"
#define R_S2H "v[104:111]"
#define R_S3L "v[112:119]"
#define R_S3H "v[120:127]"
#define R_O0 "v[128:143]"
#define R_O1 "v[144:159]"
#define R_V0A "v[160:163]"
#define R_V0B "v[164:167]"
#define R_V1A "v[168:171]"
#define R_V1B "v[172:175]"
#define R_V2A "v[176:179]"
#define R_V2B "v[180:183]"
#define R_V3A "v[184:187]"
#define R_V3B "v[188:191]"
#define R_K0M "v[192:195]"
#define R_K1M "v[196:199]"
#define R_K2M "v[200:203]"
#define R_K3M "v[204:207]"
#define R_K0R "v[208:209]"
#define R_K1R "v[210:211]"
#define R_K2R "v[212:213]"
#define R_K3R "v[214:215]"
#define R_Q0M "v[216:219]"
#define R_Q1M "v[220:223]"
#define R_Q0R "v[224:225]"
#define R_Q1R "v[226:227]"
#define QK1(s, k, q) "v_mfma_f32_32x32x16_bf16 " R_S##s ", " R_K##k##M ", " R_Q##q##M ", 0\n\t"
#define QK2(s, k, q) "v_mfma_f32_32x32x8_bf16 " R_S##s ", " R_K##k##R ", " R_Q##q##R ", " R_S##s "\n\t"
#define PV1(o, v, s) "v_smfmac_f32_32x32x32_bf16 " R_O##o ", " R_V##v##A ", " R_S##s##L ", v228\n\t"
#define PV2(o, v, s) "v_smfmac_f32_32x32x32_bf16 " R_O##o ", " R_V##v##B ", " R_S##s##H ", v228\n\t"
#define PERIOD(e, sn, k, q, sp, v, o) \
    QK1(sn, k, q) E_S##e##_0 PV1(o, v, sp) E_S##e##_1 QK2(sn, k, q) E_S##e##_2 PV2(o, v, sp) E_S##e##_3
#define LDK(k) \
    "global_load_dwordx4 " R_K##k##M ", v229, s[60:61] sc1\n\t" \
    "s_mov_b32 exec_hi, 0\n\t" \
    "global_load_dwordx2 " R_K##k##R ", v231, s[60:61] sc1\n\t"
#define LDV(v) \
    "s_mov_b64 exec, %[vmask]\n\t" \
    "global_load_dwordx4 " R_V##v##A ", v230, s[60:61] sc1\n\t" \
    "global_load_dwordx4 " R_V##v##B ", v230, s[60:61] offset:640 sc1\n\t"
#define ADV "s_mov_b64 exec, -1\n\ts_add_u32 s60, s60, 5120\n\ts_addc_u32 s61, s61, 0\n\t"
#define GROUP(n, k, v) "s_waitcnt vmcnt(" n ")\n\t" LDK(k) LDV(v) ADV
#define PERIOD_NOQK(e, sp, v, o) E_S##e##_0 PV1(o, v, sp) E_S##e##_1 E_S##e##_2 PV2(o, v, sp) E_S##e##_3
constexpr u64 VMASK = 0x000fffff000fffffull;

struct Attn { f32x32 sa, sb, oo; u32x32 vv; u32x16 km; u32x8 kr, qm; u32x4 qr, misc; const char* base; };
DEV void attn_prologue1(Attn& a, const char* kv, const char* q0, const char* q1) {
    a.base = kv - 2 * 5120;
    asm volatile(
        ".irp r,208,210,212,214\n\tv_mov_b32 v\\r, 0x3f803f80\n\t.endr\n\t"
        ".irp r,209,211,213,215\n\tv_mov_b32 v\\r, 0x3f80\n\t.endr\n\t"
        ".irp r,224,225,226,227\n\tv_mov_b32 v\\r, 0\n\t.endr\n\t"
        "global_load_dwordx4 " R_Q0M ", v229, %[q0] sc1\n\t"
        "global_load_dwordx4 " R_Q1M ", v229, %[q1] sc1\n\t"
        "s_mov_b32 exec_hi, 0\n\t"
        "global_load_dwordx2 " R_Q0R ", v231, %[q0] sc1\n\t"
        "global_load_dwordx2 " R_Q1R ", v231, %[q1] sc1\n\t"
        "s_mov_b64 exec, -1\n\t"
        LDK(0) ADV LDK(1) ADV
        : "=&{v[192:207]}"(a.km), "=&{v[208:215]}"(a.kr), "=&{v[216:223]}"(a.qm), "=&{v[224:227]}"(a.qr), "+{s[60:61]}"(a.base)
        : "{v[228:231]}"(a.misc), [q0] "s"(q0 - 10240), [q1] "s"(q1 - 10240)
        : "memory", "scc");
}
DEV void attn_init_v(Attn& a, unsigned vinit) {
    asm volatile(".irp r,160,161,162,163,164,165,166,167,168,169,170,171,172,173,174,175,176,177,178,179,180,181,182,183,184,185,186,187,188,189,190,191\n\tv_mov_b32 v\\r, %[vinit]\n\t.endr"
                 : "=&{v[160:191]}"(a.vv) : [vinit] "v"(vinit));
}
DEV void attn_init_o(Attn& a) {
    asm volatile(".irp r,128,129,130,131,132,133,134,135,136,137,138,139,140,141,142,143,144,145,146,147,148,149,150,151,152,153,154,155,156,157,158,159\n\tv_mov_b32 v\\r, 0\n\t.endr"
                 : "={v[128:159]}"(a.oo));
}
DEV void attn_init_s(Attn& a) {
    asm volatile(".irp r,112,113,114,115,116,117,118,119,120,121,122,123,124,125,126,127\n\tv_mov_b32 v\\r, 0\n\t.endr" : "={v[96:127]}"(a.sb));
}
DEV void attn_prologue2(Attn& a) {
    asm volatile(LDK(2) LDV(0) ADV LDK(3) LDV(1) ADV
                 : "+{v[160:191]}"(a.vv), "+{v[192:207]}"(a.km), "+{v[208:215]}"(a.kr), "+{s[60:61]}"(a.base)
                 : "{v[228:231]}"(a.misc), [vmask] "s"(VMASK) : "memory", "scc");
}
template <int N> DEV void attn_first_scores(Attn& a) {
    asm volatile("s_waitcnt vmcnt(%[n])\n\ts_nop 1\n\t" QK1(0, 0, 0) QK1(1, 0, 1) QK2(0, 0, 0) QK2(1, 0, 1) "s_nop 11\n\t"
                 : "=&{v[64:95]}"(a.sa), "+{v[192:207]}"(a.km), "+{v[208:215]}"(a.kr), "+{v[216:223]}"(a.qm), "+{v[224:227]}"(a.qr)
                 : [n] "i"(N) : "memory");
}
#define ITER(n0, n1, n2, n3, e0, e1, e2, e3) \
        GROUP(n0, 0, 2) e0 PERIOD(0, 2, 1, 0, 3, 3, 1) PERIOD(1, 3, 1, 1, 0, 0, 0) \
        GROUP(n1, 1, 3) e1 PERIOD(2, 0, 2, 0, 1, 0, 1) PERIOD(3, 1, 2, 1, 2, 1, 0) \
        GROUP(n2, 2, 0) e2 PERIOD(0, 2, 3, 0, 3, 1, 1) PERIOD(1, 3, 3, 1, 0, 2, 0) \
        GROUP(n3, 3, 1) e3 PERIOD(2, 0, 0, 0, 1, 2, 1) PERIOD(3, 1, 0, 1, 2, 3, 0)
#define ATTN_STATE(a) "+{v[64:95]}"(a.sa), "+{v[96:127]}"(a.sb), "+{v[128:159]}"(a.oo), "+{v[160:191]}"(a.vv), "+{v[192:207]}"(a.km), "+{v[208:215]}"(a.kr), "+{s[60:61]}"(a.base)
#define ATTN_IN(a) "{v[216:223]}"(a.qm), "{v[224:227]}"(a.qr), "{v[228:231]}"(a.misc), [vmask] "s"(VMASK)
template <int X> DEV void attn_iter(Attn& a) {
    asm volatile(ITER("%[n]", "%[n]", "4", "4", , , , ) : ATTN_STATE(a) : ATTN_IN(a), [n] "i"(4 + X) : "memory", "scc");
}
#define PF1(r, p, o) "global_load_dwordx4 " r ", %[v], " p o " sc1\n\t"
#define PF2(r0, r1, p) PF1(r0, p, "") PF1(r1, p, " offset:1024")
#define PFA "s_add_u32 s62, s62, 69632\n\ts_addc_u32 s63, s63, 0\n\t"
#define PFB "s_add_u32 s64, s64, 69632\n\ts_addc_u32 s65, s65, 0\n\t"
DEV void attn_last(Attn& a, u32x32& pfa, u32x32& pfb, u32x8& pfc, const char* pa, const char* pb, unsigned voff) {
    asm volatile(
        ITER("4", "5", "6", "6", PF1("v[0:3]", "s[62:63]", ""), PF1("v[16:19]", "s[62:63]", " offset:1024") PFA, PF1("v[4:7]", "s[62:63]", ""), PF1("v[20:23]", "s[62:63]", " offset:1024") PFA)
        "s_setprio 1\n\t"
        ITER("6", "6", "6", "6", PF1("v[8:11]", "s[62:63]", ""), PF1("v[24:27]", "s[62:63]", " offset:1024") PFA, PF1("v[12:15]", "s[62:63]", ""), PF1("v[28:31]", "s[62:63]", " offset:1024"))
        "s_waitcnt vmcnt(6)\n\t" LDV(2) ADV PF2("v[32:35]", "v[48:51]", "s[64:65]") PFB
        PERIOD(0, 2, 1, 0, 3, 3, 1) LDV(3) ADV
        PF2("v[36:39]", "v[52:55]", "s[64:65]") PFB
        PERIOD(1, 3, 1, 1, 0, 0, 0)
        "s_waitcnt vmcnt(9)\n\t"
        PF2("v[40:43]", "v[56:59]", "s[64:65]") PFB
        PERIOD(2, 0, 2, 0, 1, 0, 1)
        PF2("v[44:47]", "v[60:63]", "s[64:65]") PFB
        PERIOD(3, 1, 2, 1, 2, 1, 0)
        "s_setprio 0\n\ts_waitcnt vmcnt(6)\n\t"
        PF2("v[232:235]", "v[236:239]", "s[64:65]")
        PERIOD(0, 2, 3, 0, 3, 1, 1)
        PERIOD(1, 3, 3, 1, 0, 2, 0)
        PERIOD_NOQK(2, 1, 2, 1) PERIOD_NOQK(3, 2, 3, 0)
        "s_nop 1\n\t" PV1(1, 3, 3) PV2(1, 3, 3) "s_nop 15\n\t"
        : ATTN_STATE(a), "=&{v[0:31]}"(pfa), "=&{v[32:63]}"(pfb), "=&{v[232:239]}"(pfc), "+{s[62:63]}"(pa), "+{s[64:65]}"(pb)
        : ATTN_IN(a), [v] "v"(voff)
        : "memory", "scc");
}
template <int O, class V> DEV float max16(const V& s) {
    float a = fmaxf(fmaxf(s[O], s[O + 1]), s[O + 2]), b = fmaxf(fmaxf(s[O + 3], s[O + 4]), s[O + 5]);
    a = fmaxf(fmaxf(a, s[O + 6]), s[O + 7]); b = fmaxf(fmaxf(b, s[O + 8]), s[O + 9]);
    a = fmaxf(fmaxf(a, s[O + 10]), s[O + 11]); b = fmaxf(fmaxf(b, s[O + 12]), s[O + 13]);
    a = fmaxf(fmaxf(a, s[O + 14]), s[O + 15]);
    return fmaxf(a, b);
}
DEV void attn_set_reference(Attn& a, float (&c)[2], int hh) {
#pragma unroll
    for (int i = 0; i < 2; ++i) {
        c[i] = swap_max(c[i]);
        const u32x2 t = split3(-c[i]);
        if (hh) { a.qr[2 * i] = t[0]; a.qr[2 * i + 1] = t[1]; }
    }
}

struct Ring {
    const char *x, *wq, *wgu;
    unsigned xo0, xo1, wo0, wo1, go0, go1, go2;
    unsigned lds0; int w, s0;
};
template <int C, int P> DEV void ring_piece(const Ring& r) {
    if constexpr (C < 20) {
        int cc = r.s0 + C % 10; cc = cc >= 10 ? cc - 10 : cc;
        if constexpr (C < 10) {
            const unsigned d = r.lds0 + (C & 3) * 32768 + r.w * 2048;
            if constexpr (P == 0) glds16(r.xo0, r.x + cc * 128, d);
            if constexpr (P == 1) glds16(r.xo1, r.x + cc * 128, d + 1024);
            if constexpr (P == 2) glds16(r.wo0, r.wq + cc * (64 * 3840), d + 16384);
            if constexpr (P == 3) glds16(r.wo1, r.wq + cc * (64 * 3840), d + 16384 + 1024);
        } else {
            const unsigned d = r.lds0 + (C & 3) * 32768 + r.w * 3072 + (r.w >> 2) * 32;
            const char* b = r.wgu + cc * (64 * 3072);
            if constexpr (P == 0) glds16(r.go0, b, d);
            if constexpr (P == 1) glds16(r.go1, b, d + 1024);
            if constexpr (P == 2) glds16(r.go2, b, d + 2048);
        }
    }
}
template <int C> DEV void ring_issue(const Ring& r) { ring_piece<C, 0>(r); ring_piece<C, 1>(r); ring_piece<C, 2>(r); ring_piece<C, 3>(r); }
constexpr int ring_n(int c) { return c < 10 ? 4 : (c < 20 ? 3 : 0); }
constexpr int vm_pre(int c) { return c == 6 ? 4 : (c == 15 ? 5 : 0); }
constexpr int vm_post(int c) { return (c >= 0 && c <= 4 ? 1 : 0) + ((c == 2 || c == 11) ? 1 : 0) + ((c == 9 || c == 12 || c == 16) ? 4 : 0) + (c == 17 ? 8 : 0); }
constexpr int vm_wait(int c) {
    if (c == 11) return ring_n(14);
    return vm_post(c - 3) + vm_pre(c - 2) + ring_n(c + 2) + vm_post(c - 2) + vm_pre(c - 1) + ring_n(c + 3) + vm_post(c - 1);
}

__global__ __launch_bounds__(512, 2) void fused_block(const char* x, const char* wqkv, const char* wo, const char* wg, const char* wu, const char* wd,
                                                      char* out, char* ws) {
    __shared__ __attribute__((aligned(16))) char smem[L_TOTAL];
    LAS char* lds = (LAS char*)smem;
    const int w = __builtin_amdgcn_readfirstlane(threadIdx.x >> 6);
    const int bid = blockIdx.x, xcd = bid & 7, rr = bid >> 3;
#define LANE_IDS int lane = __builtin_amdgcn_mbcnt_hi(~0u, __builtin_amdgcn_mbcnt_lo(~0u, 0u)); asm volatile("" : "+v"(lane)); \
    const int tid = 64 * w + lane, r32 = lane & 31, hh = lane >> 5; (void)tid; (void)r32; (void)hh
    asm volatile("" : "+s"(x), "+s"(wqkv), "+s"(wo), "+s"(wg), "+s"(wu), "+s"(wd), "+s"(out), "+s"(ws));
    const __amdgpu_buffer_rsrc_t wsr = __builtin_amdgcn_make_buffer_rsrc(ws, 0, WS_END, 0x00020000);
    LAS unsigned* lflag = (LAS unsigned*)(lds + L_FLAG);
    u64 tag;
    STAMP_DECL;
    STAMP(0);
    const int mi = rr & 7, ji = rr >> 3, m = 8 * (xcd & 1) + mi, j = 4 * (xcd >> 1) + ji;
    const int wm = w & 3, wn = w >> 2;
    const int g = 4 * xcd + (rr >> 3), mq = rr & 7, qq = w & 3, kh = w >> 2, odd = g & 1;
    const char* qp = ws + WS_Q + ((g >> 1) * 64 + 8 * mq + 2 * qq) * 2560;
    const unsigned kvoff = WS_KV + ((g >> 1) * 64 + 32 * kh) * 5120;
    Attn at;
    float c[2];
    {
        LANE_IDS;
        Ring rg;
        rg.x = x + m * 128 * 1280; rg.wq = wqkv + (j < 15 ? j : 14) * 256; rg.wgu = (w < 4 ? wg : wu) + j * 192;
        rg.lds0 = (unsigned)(size_t)lds; rg.w = w;
        rg.s0 = (((5 * ji + 1) >> 1) + mi + (mi >> 2)) % 10;
        rg.xo0 = (16 * w + (lane >> 3)) * 1280 + (((lane & 7) ^ (lane >> 4)) << 4);
        rg.xo1 = (16 * w + 8 + (lane >> 3)) * 1280 + (((lane & 7) ^ (4 + (lane >> 4))) << 4);
        rg.wo0 = (8 * w + (lane >> 4)) * 3840 + (((lane & 15) ^ ((lane >> 4) << 2)) << 4);
        rg.wo1 = rg.wo0 + 4 * 3840;
        ring_issue<0>(rg); __builtin_amdgcn_s_barrier();
        ring_issue<1>(rg);
        asm volatile("s_load_dwordx2 %0, %1, 0x0\n\ts_waitcnt lgkmcnt(0)" : "=s"(tag) : "s"(ws) : "memory");
        tag += 1;
        const unsigned tag32 = (unsigned)tag;
        { const int p0 = (w & 3) * 192 + lane, p1 = p0 + 64, p2 = p0 + 128;
          rg.go0 = (p0 / 12) * 3072 + (p0 % 12) * 16; rg.go1 = (p1 / 12) * 3072 + (p1 % 12) * 16; rg.go2 = (p2 / 12) * 3072 + (p2 % 12) * 16; }
        if (tid < 4) lflag[tid] = 0u;

        const int g4 = lane >> 4, q4 = (lane >> 2) & 3, p4 = lane & 3, rot = wm ^ (2 * wn);
        const int xrow = 32 * wm + r32;
        const unsigned xa = (xrow * 128 + ((hh ^ ((xrow >> 1) & 7)) << 4)) ^ (rot << 5);
        const unsigned wab = 16384 + (rot << 12) + (8 * (g4 >> 1) + q4) * 256 + (g4 & 1) * 32 + (p4 & 1) * 16 + (p4 >> 1) * 8;
        const unsigned wa0 = wab + (((2 * wn) ^ q4) << 6), wa1 = wab + (((2 * wn + 1) ^ q4) << 6);
        LAS float* part = (LAS float*)(lds + L_PART); LAS u32x2* fin = (LAS u32x2*)(lds + L_FIN);
        bf16x8 xf[40], fw[2][3];
        f32x16 acc[2] = {};
        float rs = 0.f, rq = 0.f, cs0 = 0.f, cs1 = 0.f, pv[4];
        u32x2 gr[4], fa;
        const unsigned col2 = (mi & 3) == 3 ? 0x3f803f80u : 0u;
        int pw_row = (2 * wn + hh) * 384 + xrow, pw_col = wm * 384 + 256 + 64 * wn + lane, pr_i = w < 6 ? tid : lane;
        unsigned pub_o = tid * 8 + (w < 6 ? WS_GA + bid * 3072 : OOB), warm_o = min(lane, 47) * 16;
        asm volatile("" : "+v"(pw_row), "+v"(pw_col), "+v"(pr_i), "+v"(pub_o), "+v"(warm_o), "+v"(rg.go0), "+v"(rg.go1), "+v"(rg.go2));
        const char* warm_p = rg.wgu - ji * 192 + (4 * rr + wm) * (5 * 3072);
        const unsigned gab = w < 4 ? WS_GA + (xcd + 8 * mi) * 3072 + w * 512 : WS_GA + (xcd + 64 * ji + 32 * ((w >> 1) & 1)) * 3072 + 2048 + (w & 1) * 512;
        const unsigned gao = gab + lane * 8, gas = w < 4 ? 64 * 3072 : 8 * 3072;
#define RW1(f, b, c, k) fw[f][b] = ldstr(lds + ((c) & 3) * 32768 + ((b ? wa1 : wa0) ^ ((k) << 12)), lds + ((c) & 3) * 32768 + ((b ? wa1 : wa0) ^ ((k) << 12)) + 1024)
#define RX1(c, k) xf[4 * (c) + (k)] = lds128(lds + ((c) & 3) * 32768 + (xa ^ ((k) << 5)))
#define M1(f, b, c, k) acc[b] = MFMA32(fw[f][b], xf[4 * (c) + (k)], acc[b]); SBAR()
        STAMPP(2, 1);
        wait_bar<4>();
        STAMPP(2, 2);
        RW1(0, 0, 0, 0); RX1(0, 0); RW1(0, 1, 0, 0); RW1(1, 0, 0, 1); RX1(0, 1); RW1(1, 1, 0, 1); SBAR();
        ring_issue<2>(rg); ring_piece<3, 0>(rg); SBAR();
#define A1C(c, H3, H4, H5, H6) do { constexpr int n_ = c < 9 ? c + 1 : 0; \
        M1(0, 0, c, 0); RW1(0, 0, c, 2); RX1(c, 2); ring_piece<c + 3, 1>(rg); SBAR(); \
        M1(0, 1, c, 0); RW1(0, 1, c, 2); ring_piece<c + 3, 2>(rg); SBAR(); \
        M1(1, 0, c, 1); RW1(1, 0, c, 3); RX1(c, 3); ring_piece<c + 3, 3>(rg); SBAR(); \
        M1(1, 1, c, 1); RW1(1, 1, c, 3); H3; SBAR(); \
        M1(0, 0, c, 2); H4; SBAR(); M1(0, 1, c, 2); H5; SBAR(); \
        wait_bar<vm_wait(c)>(); \
        M1(1, 0, c, 3); \
        if constexpr (c < 9) { RW1(0, 0, n_, 0); RX1(n_, 0); RW1(0, 1, n_, 0); RW1(1, 0, n_, 1); RX1(n_, 1); } \
        H6; SBAR(); \
        M1(1, 1, c, 3); \
        if constexpr (c < 9) { RW1(1, 1, n_, 1); ring_piece<c + 4, 0>(rg); } else ring_issue<c + 4>(rg); \
        SBAR(); } while (0)
#define WARM(i) glds16(warm_o, warm_p + (i) * 3072, rg.lds0 + L_TR + w * 2048)
        A1C(0, , , ({ rs = dot_ones(xf[0], rs); rq = dot_self(xf[0], rq); rs = dot_ones(xf[1], rs); rq = dot_self(xf[1], rq);
                      cs0 = dot_ones(fw[0][0], cs0); cs1 = dot_ones(fw[0][1], cs1); }), );
        STAMPP(2, 3);
        A1C(1, WARM(0), , ({ rs = dot_ones(xf[4], rs); rq = dot_self(xf[4], rq); rs = dot_ones(xf[5], rs); rq = dot_self(xf[5], rq);
                      cs0 = dot_ones(fw[0][0], cs0, col2); cs1 = dot_ones(fw[0][1], cs1, col2); }), );
        STAMPP(2, 4);
        A1C(2, WARM(1), ({ cs0 = swap_add(cs0); cs1 = swap_add(cs1); part[pw_col] = hh ? cs1 : cs0; }),
            ({ float r2 = dot_ones(xf[8], rs), q2 = dot_self(xf[8], rq); r2 = dot_ones(xf[9], r2); q2 = dot_self(xf[9], q2);
               part[pw_row] = (ji & 1) ? rs : r2; part[pw_row + 128] = (ji & 1) ? rq : q2; }),
            ({ pv[0] = part[pr_i]; pv[1] = part[384 + pr_i]; pv[2] = part[768 + pr_i]; pv[3] = part[1152 + pr_i]; }));
        STAMPP(2, 5);
        A1C(3, WARM(2), __builtin_amdgcn_raw_buffer_store_b64((u32x2){__float_as_uint((pv[0] + pv[1]) + (pv[2] + pv[3])), tag32}, wsr, pub_o, 0, SC1), , );
        STAMPP(2, 6); A1C(4, WARM(3), , , ); STAMPP(2, 7); A1C(5, WARM(4), , , ); STAMPP(2, 8);
        A1C(6, , , , ({ gr[0] = ld2_async(ws, gao); gr[1] = ld2_async(ws, gao + gas); gr[2] = ld2_async(ws, gao + 2 * gas); gr[3] = ld2_async(ws, gao + 3 * gas); }));
        STAMPP(2, 9); A1C(7, , , , ); STAMPP(2, 10);
        float rstd, st0, st1;
        u32x2 nmu, cf[3];
        A1C(8, ({ asm volatile("s_waitcnt vmcnt(6)" : "+v"(gr[0]), "+v"(gr[1]), "+v"(gr[2]), "+v"(gr[3]) :: "memory");
                  st0 = gran_sum(ws, gao, gas, gr, tag32); }),
            ({ u32x2 t = split3(st0); if (w < 4) t[0] = __float_as_uint(st0);
               fin[(w < 6 ? 64 * w : 64 * w + 64) + lane] = t; }), ,
            ({ st0 = *(const LAS float*)(fin + xrow); st1 = *(const LAS float*)(fin + 128 + xrow);
               cf[0] = fin[256 + 192 * hh + 64 * wn + r32]; cf[1] = fin[256 + 192 * hh + 64 * wn + 32 + r32]; }));
        A1C(9, ({ const float mean = st0 * (1.f / 640.f), var = fmaxf(st1 * (1.f / 640.f) - mean * mean, 0.f);
                  nmu = (u32x2){pk2(-mean, -mean), pk2(-mean, 0.f)};
                  rstd = __builtin_amdgcn_rcpf(bfr(__builtin_amdgcn_sqrtf(bfr(bfr(var) + 1e-5f))));
                  asm volatile("" : "+v"(nmu), "+v"(rstd)); }),
            , ({ acc[0] = MFMA8(cf[0], nmu, acc[0]); acc[1] = MFMA8(cf[1], nmu, acc[1]); }), );
        STAMPP(2, 11);
        STAMPP(1, 1);
        { const int tile = 4 * m + wm;
          const float sc = j < 5 ? rstd * QSCALE : rstd;
          u32x4 y[4];
#pragma unroll
          for (int i = 0; i < 4; ++i)
#pragma unroll
              for (int e = 0; e < 4; ++e) y[i][e] = pk2(sc * acc[i >> 1][8 * (i & 1) + 2 * e], sc * acc[i >> 1][8 * (i & 1) + 2 * e + 1]);
          if (j < 10) {
              const unsigned base = (j < 5 ? WS_Q + tile * 2560 : WS_KV + tile * 5120) + r32 * 16, ps = j < 5 ? 64 * 2560 : 64 * 5120;
              const int c8 = (j < 5 ? j : j - 5) * 16 + 8 * wn + hh;
#pragma unroll
              for (int i = 0; i < 4; ++i) {
                  const int ci = c8 + 2 * i, pr = ci / 5, pl = ci - 5 * pr;
                  __builtin_amdgcn_raw_buffer_store_b128(y[i], wsr, base + pr * ps + pl * 512, 0, SC1);
              }
          } else {
              LAS char* tp = lds + L_TR + w * 2048;
              const int cu = 2 * (g4 & 1) + (p4 >> 1);
#pragma unroll
              for (int b = 0; b < 2; ++b) {
#pragma unroll
                  for (int v = 0; v < 2; ++v) *(LAS u32x4*)(tp + r32 * 64 + (((2 * v + hh) ^ ((r32 >> 1) & 3)) << 4)) = y[2 * b + v];
                  asm volatile("s_waitcnt lgkmcnt(0)" ::: "memory");
                  const int col = (j - 10) * 128 + 64 * wn + 32 * b + r32, head = col / 20, d = col - 20 * head;
                  const unsigned base = j < 15 ? WS_KV + ((head >> 1) * 64 + tile) * 5120 + 2560 + (head & 1) * 1280 + d * 16 + hh * 320 : OOB;
#pragma unroll
                  for (int i = 0; i < 2; ++i) {
                      const int r0 = 16 * i + 8 * hh + q4;
                      const bf16x8 v = ldstr(tp + r0 * 64 + ((cu ^ (q4 >> 1)) << 4) + (p4 & 1) * 8,
                                             tp + (r0 + 4) * 64 + ((cu ^ (2 + (q4 >> 1))) << 4) + (p4 & 1) * 8);
                      __builtin_amdgcn_raw_buffer_store_b128(__builtin_bit_cast(u32x4, v), wsr, base + i * 640, 0, SC1);
                  }
                  asm volatile("s_waitcnt lgkmcnt(0)" ::: "memory");
              }
          } }
        STAMPP(1, 2); STAMPP(4, 1);
        unsigned ga[4];
#pragma unroll
        for (int ks = 0; ks < 4; ++ks) ga[ks] = (g4 & 1) * (12288 + 32) + (8 * (g4 >> 1) + q4) * 192 + wn * 96 + p4 * 8 + (ks ^ rot) * 3072;
        float cs[3] = {0.f, 0.f, 0.f};
        f32x16 ac2[3] = {};
#define RW2(f, b, c, k) fw[f][b] = ldstr(lds + ((c) & 3) * 32768 + ga[k] + b * 32, lds + ((c) & 3) * 32768 + ga[k] + b * 32 + 768)
#define M2(f, b, c, k) ac2[b] = MFMA32(fw[f][b], xf[4 * ((c) - 10) + (k)], ac2[b]); SBAR()
        SBAR(); RW2(0, 0, 10, 0); RW2(0, 1, 10, 0); RW2(0, 2, 10, 0); RW2(1, 0, 10, 1); RW2(1, 1, 10, 1); RW2(1, 2, 10, 1); SBAR();
#define A2C(c, H0, H1, H2, H3, H5, H6) do { \
        M2(0, 0, c, 0); RW2(0, 0, c, 2); H0; SBAR(); \
        M2(0, 1, c, 0); RW2(0, 1, c, 2); H1; SBAR(); \
        M2(0, 2, c, 0); RW2(0, 2, c, 2); H2; SBAR(); \
        M2(1, 0, c, 1); RW2(1, 0, c, 3); H3; SBAR(); \
        M2(1, 1, c, 1); RW2(1, 1, c, 3); SBAR(); \
        M2(1, 2, c, 1); RW2(1, 2, c, 3); SBAR(); \
        M2(0, 0, c, 2); M2(0, 1, c, 2); H5; SBAR(); \
        if constexpr (c < 19) wait_bar<vm_wait(c)>(); \
        M2(0, 2, c, 2); \
        if constexpr (c < 19) { RW2(0, 0, c + 1, 0); RW2(0, 1, c + 1, 0); RW2(0, 2, c + 1, 0); } \
        H6; SBAR(); \
        M2(1, 0, c, 3); if constexpr (c < 19) RW2(1, 0, c + 1, 1); ring_piece<c + 4, 0>(rg); SBAR(); \
        M2(1, 1, c, 3); if constexpr (c < 19) RW2(1, 1, c + 1, 1); ring_piece<c + 4, 1>(rg); SBAR(); \
        M2(1, 2, c, 3); if constexpr (c < 19) RW2(1, 2, c + 1, 1); ring_piece<c + 4, 2>(rg); SBAR(); STAMPP(4, c - 8); } while (0)
        A2C(10, , , , , ({ cs[0] = dot_ones(fw[0][0], cs[0]); cs[1] = dot_ones(fw[0][1], cs[1]); cs[2] = dot_ones(fw[0][2], cs[2]); }), );
        A2C(11, , , , , ({ cs[0] = dot_ones(fw[0][0], cs[0], col2); cs[1] = dot_ones(fw[0][1], cs[1], col2); cs[2] = dot_ones(fw[0][2], cs[2], col2);
                           cs[0] = swap_add(cs[0]); cs[1] = swap_add(cs[1]); cs[2] = swap_add(cs[2]);
                           part[wm * 384 + 96 * wn + lane] = hh ? cs[1] : cs[0]; part[wm * 384 + 96 * wn + 64 + r32] = cs[2]; }),
            ({ const int pi = w < 3 ? tid : lane; pv[0] = part[pi]; pv[1] = part[384 + pi]; pv[2] = part[768 + pi]; pv[3] = part[1152 + pi]; }));
        if (tid == 0) flag_store(ws + WS_FA + bid * 8, tag);
        A2C(12, __builtin_amdgcn_raw_buffer_store_b64((u32x2){__float_as_uint((pv[0] + pv[1]) + (pv[2] + pv[3])), tag32}, wsr, tid * 8 + (w < 3 ? WS_GB + bid * 1536 : OOB), 0, SC1), , , , , );
        const int unit = bid + 256 * w, us = unit / 10, nb = unit - 10 * us;
        const char* psrc = us < 48 ? wd : wo;
        const u32x4 psrd = {(unsigned)(size_t)psrc, (unsigned)((size_t)psrc >> 32) & 0xffffu, unit < 680 ? (us < 48 ? 1536u * 1280u : 640u * 1280u) : 0u, 0x00020000u};
        const unsigned plds = L_TR + min(w, 3) * 4096, pvo = (hh * (us < 64 ? 8 : 20) + ((lane >> 3) & 3)) * 1280 + (lane & 7) * 16;
#define PLD(i) ({ const int kap = 4 * us + i, r0 = us < 48 ? 16 * (kap >> 1) + 4 * (kap & 1) : (us < 64 ? 20 * ((kap >> 1) - 96) + 4 * (kap & 1) : 40 * (kap - 256) + 16); \
            asm volatile("s_mov_b32 m0, %3\n\ts_nop 0\n\tbuffer_load_dwordx4 %0, %1, %2 offen lds" :: "v"(pvo), "s"(psrd), "s"(r0 * 1280 + nb * 128), "s"(rg.lds0 + plds + i * 1024) : "memory"); })
        A2C(13, PLD(0), PLD(1), PLD(2), PLD(3), , );
        A2C(14, , , , , , );
        const int gbh = w >= 3 && w < 6;
        const unsigned gbo = WS_GB + (xcd + 64 * ji + 32 * gbh) * 1536 + (w < 6 ? w - 3 * gbh : 0) * 512 + lane * 8, fao = WS_FA + (tid & 255) * 8;
        A2C(15, , , , , , ({ gr[0] = ld2_async(ws, gbo); gr[1] = ld2_async(ws, gbo + 8 * 1536); gr[2] = ld2_async(ws, gbo + 16 * 1536); gr[3] = ld2_async(ws, gbo + 24 * 1536);
                   fa = ld2_async(ws, fao); }));
        A2C(16, , , , , , );
        const unsigned pdst = lane * 16 + (unit < 680 ? WS_WP + (4 * nb * 68 + us) * 1024 : OOB);
        const LAS char* prd = lds + plds + (8 * g4 + q4) * 128 + p4 * 8;
        bf16x8 pk[4];
#define PST(i) __builtin_amdgcn_raw_buffer_store_b128(__builtin_bit_cast(u32x4, pk[i]), wsr, pdst + i * (68 * 1024), 0, SC1)
        A2C(17, ({ asm volatile("s_waitcnt vmcnt(3)" : "+v"(gr[0]), "+v"(gr[1]), "+v"(gr[2]), "+v"(gr[3]), "+v"(fa) :: "memory");
                   st0 = gran_sum(ws, gbo, 8 * 1536, gr, tag32);
                   flag_wait(ws, fao, fa, tag); }),
            ({ fin[256 + (w < 6 ? tid : lane)] = split3(st0);
               pk[0] = ldstr(prd, prd + 512); pk[1] = ldstr(prd + 32, prd + 544); }),
            ({ pk[2] = ldstr(prd + 64, prd + 576); pk[3] = ldstr(prd + 96, prd + 608); }), ,
            ({ PST(0); PST(1); PST(2); PST(3); }),
            ({ cf[0] = fin[256 + 192 * hh + 96 * wn + r32]; cf[1] = fin[256 + 192 * hh + 96 * wn + 32 + r32]; cf[2] = fin[256 + 192 * hh + 96 * wn + 64 + r32]; }));
        A2C(18, at.misc = ((u32x4){0xddddu, 10240u + odd * 1536 + lane * 16, 2560u + odd * 1280 + hh * 320 + r32 * 16, 10240u + 1024 + r32 * 16 + odd * 8}),
            attn_prologue1(at, ws + kvoff, qp + kh * 2560, qp + (kh ^ 1) * 2560), ,
            ({ ac2[0] = MFMA8(cf[0], nmu, ac2[0]); ac2[1] = MFMA8(cf[1], nmu, ac2[1]); ac2[2] = MFMA8(cf[2], nmu, ac2[2]); }), , );
        A2C(19, attn_init_v(at, r32 == 20 ? 0x3f803f80u : 0u),
            attn_prologue2(at), attn_init_o(at), attn_init_s(at), , );
        STAMPP(1, 3);
        __builtin_amdgcn_s_setprio(3);
        attn_first_scores<10>(at);
        { const unsigned base = WS_HP + (8 * m + 2 * wm + (r32 >> 4)) * (272 * 256) + (2 * (6 * j + 3 * wn) + hh) * 256 + (r32 & 15) * 16;
          const float r2 = rstd * rstd;
#pragma unroll
          for (int b = 0; b < 3; ++b) {
              u32x4 v;
#pragma unroll
              for (int e = 0; e < 4; ++e) v[e] = pk2(ac2[b][2 * e] * r2 * ac2[b][8 + 2 * e], ac2[b][2 * e + 1] * r2 * ac2[b][9 + 2 * e]);
              __builtin_amdgcn_raw_buffer_store_b128(v, wsr, base + b * 512, 0, SC1);
          } }
        STAMPP(1, 4); STAMPP(3, 1);
        c[0] = max16<0>(at.sa); c[1] = max16<16>(at.sa);
    }
    const int rb = 4 * xcd + (rr >> 3), ct = rr & 7;
    const unsigned aob = WS_HP + 4 * rb * (272 * 256), bob = WS_WP + 5 * ct * (68 * 1024);
    const int krot = 2 * (((rr >> 3) + ct) % 3), k2 = 6 * w + (krot + 2) % 6, k4 = 6 * w + (krot + 4) % 6;
    u32x32 pfa, pfb; u32x8 pfc;
#pragma unroll 1
    for (int attempt = 0;; ++attempt) {
        LANE_IDS;
        attn_set_reference(at, c, hh);
        asm volatile("" : "+v"(c[0]), "+v"(c[1]));
        attn_first_scores<63>(at);
        at.base = ws + kvoff + 2 * 5120;
        STAMPP(1, 5); STAMPP(3, 2);
        attn_iter<3>(at); STAMPP(3, 3);
        arrive_publish(lflag + 1, lane, ws + WS_FH + bid * 8, tag);
        __builtin_amdgcn_s_setprio(2);
        attn_iter<0>(at); STAMPP(3, 4);
        attn_iter<0>(at); STAMPP(3, 5);
        u32x4 fh0 = ld4_async(ws, WS_FH + lane * 16), fh1 = ld4_async(ws, WS_FH + 1024 + lane * 16);
        attn_iter<2>(at); STAMPP(3, 6);
        asm volatile("" : "+v"(fh0), "+v"(fh1));
        flag_wait4(ws, WS_FH + lane * 16, fh0, fh1, tag);
        if (bid == 0 && tid == 0) flag_store(ws + WS_EPOCH, tag);
        { const unsigned li = (rr * 8 + w) * 25 + min(lane, 24), lt = li / 160;
          glds4(WS_WP + lt * (68 * 1024) + 48 * 1024 + (li - 160 * lt) * 128, ws, (unsigned)(size_t)lds + L_TR + w * 256); }
        STAMPP(3, 7);
        attn_iter<1>(at); STAMPP(3, 8); STAMPP(3, 9);
        attn_last(at, pfa, pfb, pfc, ws + aob + (6 * w + krot) * 1024, ws + bob + (6 * w + krot) * 1024, lane * 16);
        STAMPP(1, 6); STAMPP(3, 10);
        { int ln = __builtin_amdgcn_mbcnt_hi(~0u, __builtin_amdgcn_mbcnt_lo(~0u, 0u)); asm volatile("" : "+v"(ln));
          LAS float* snd = (LAS float*)lds + w * 13 * 64 + ln;
          float chk0 = (at.oo[8] * 0x1p30f) * 0.f, chk1 = (at.oo[24] * 0x1p30f) * 0.f;
#pragma unroll
          for (int i = 0; i < 12; ++i) { chk0 = __builtin_fmaf(at.oo[i], 0.f, chk0); chk1 = __builtin_fmaf(at.oo[16 + i], 0.f, chk1); }
          if (!(chk0 + chk1 == 0.f)) lflag[0] = 1u;
#pragma unroll
          for (int i = 0; i < 12; ++i) snd[i * 64] = at.oo[16 + i];
          snd[12 * 64] = c[1]; }
        lds_bar();
        const unsigned redo = lflag[0];
        { int lane = __builtin_amdgcn_mbcnt_hi(~0u, __builtin_amdgcn_mbcnt_lo(~0u, 0u)); asm volatile("" : "+v"(lane));
          const int r32 = lane & 31, hh = lane >> 5;
          float f[12];
          const LAS float* rcv = (const LAS float*)lds + (w ^ 4) * 13 * 64 + lane;
          const float co = rcv[12 * 64], cx = fmaxf(c[0], co) + 1.f;
          const float wa = __builtin_amdgcn_exp2f(c[0] - cx), wb = __builtin_amdgcn_exp2f(co - cx);
#pragma unroll
          for (int i = 0; i < 12; ++i) f[i] = at.oo[i] * wa + rcv[i * 64] * wb;
          const float inv = __builtin_amdgcn_rcpf(swap_add(hh ? f[8] : 0.f));
          const unsigned base = WS_HP + (16 * mq + 4 * qq + 2 * kh + (r32 >> 4)) * (272 * 256) + (r32 & 15) * 16;
          const u32x4 v = {pk2(f[0] * inv, f[1] * inv), pk2(f[2] * inv, f[3] * inv), pk2(f[4] * inv, f[5] * inv), pk2(f[6] * inv, f[7] * inv)};
          __builtin_amdgcn_raw_buffer_store_b128(v, wsr, base + (192 + 2 * g + hh) * 256, 0, SC1);
          const u32x2 v2 = {pk2(f[8] * inv, f[9] * inv), pk2(f[10] * inv, f[11] * inv)};
          __builtin_amdgcn_raw_buffer_store_b64(v2, wsr, hh ? OOB : base + (256 + (g >> 1)) * 256 + (g & 1) * 8, 0, SC1); }
        if (attempt || __builtin_amdgcn_readfirstlane(redo) == 0u) break;
        lds_bar();
        asm volatile("s_waitcnt vmcnt(0)" :: "{v[0:31]}"(pfa), "{v[32:63]}"(pfb), "{v[232:239]}"(pfc) : "memory");
        { int ln = __builtin_amdgcn_mbcnt_hi(~0u, __builtin_amdgcn_mbcnt_lo(~0u, 0u)); asm volatile("" : "+v"(ln));
          attn_prologue1(at, ws + kvoff, qp + kh * 2560, qp + (kh ^ 1) * 2560); attn_init_v(at, (ln & 31) == 20 ? 0x3f803f80u : 0u);
          attn_prologue2(at); attn_init_o(at); attn_init_s(at); }
        attn_first_scores<0>(at);
        c[0] = -INFINITY; c[1] = -INFINITY;
#pragma unroll 1
        for (int t = 0; t < 32; ++t) {
            const u32x4 km = __builtin_amdgcn_raw_buffer_load_b128(wsr, at.misc[1] - 10240, kvoff + t * 5120, SC1);
            const u32x2 kr = __builtin_amdgcn_raw_buffer_load_b64(wsr, at.misc[3] - 10240, kvoff + t * 5120, SC1);
#pragma unroll
            for (int i = 0; i < 2; ++i) {
                const u32x4 qm = {at.qm[4 * i], at.qm[4 * i + 1], at.qm[4 * i + 2], at.qm[4 * i + 3]};
                const u32x2 qr = {at.qr[2 * i], at.qr[2 * i + 1]};
                f32x16 s = MFMA32(__builtin_bit_cast(bf16x8, km), __builtin_bit_cast(bf16x8, qm), (f32x16){});
                s = MFMA8(kr, qr, s);
                c[i] = fmaxf(c[i], max16<0>(s));
            }
        }
    }
    LANE_IDS;
    auto loada = [&](int s, u32x4 (&a)[4]) {
#pragma unroll
        for (int i = 0; i < 4; ++i) a[i] = __builtin_amdgcn_raw_buffer_load_b128(wsr, lane * 16, aob + s * 1024 + i * (272 * 256), SC1);
    };
    auto loadb = [&](int s, u32x4 (&b)[5]) {
#pragma unroll
        for (int i = 0; i < 5; ++i) b[i] = __builtin_amdgcn_raw_buffer_load_b128(wsr, lane * 16, bob + s * 1024 + i * (68 * 1024), SC1);
    };
    STAMPP(1, 7); STAMPP(3, 11);

    f32x4 acc[4][5] = {};
    auto mma = [&](const u32x4 (&a)[4], const u32x4 (&b)[5]) {
#pragma unroll
        for (int i = 0; i < 4; ++i)
#pragma unroll
            for (int n = 0; n < 5; ++n) acc[i][n] = MFMA16(__builtin_bit_cast(bf16x8, b[n]), __builtin_bit_cast(bf16x8, a[i]), acc[i][n]);
    };
    u32x4 a0[4], b0[5], a1[4], b1[5], a2[4], b2[5], a3[4], b3[5];
    SBAR();
    asm volatile("s_waitcnt vmcnt(2)" : "+{v[0:31]}"(pfa), "+{v[32:63]}"(pfb), "+{v[232:239]}"(pfc) :: "memory");
#pragma unroll
    for (int i = 0; i < 4; ++i) {
        a0[i] = (u32x4){pfa[4 * i], pfa[4 * i + 1], pfa[4 * i + 2], pfa[4 * i + 3]}; a1[i] = (u32x4){pfa[16 + 4 * i], pfa[17 + 4 * i], pfa[18 + 4 * i], pfa[19 + 4 * i]};
        b0[i] = (u32x4){pfb[4 * i], pfb[4 * i + 1], pfb[4 * i + 2], pfb[4 * i + 3]}; b1[i] = (u32x4){pfb[16 + 4 * i], pfb[17 + 4 * i], pfb[18 + 4 * i], pfb[19 + 4 * i]};
    }
    b0[4] = (u32x4){pfc[0], pfc[1], pfc[2], pfc[3]}; b1[4] = (u32x4){pfc[4], pfc[5], pfc[6], pfc[7]};
    mma(a0, b0); SBAR();
    asm volatile("s_waitcnt vmcnt(0)" ::: "memory");
    arrive_publish(lflag + 2, lane, ws + WS_FO + (mq * 32 + g) * 128, tag);
    SBAR();
    loada(k2, a2); loadb(k2, b2); loada(k2 + 1, a3); loadb(k2 + 1, b3); loada(k4, a0); loadb(k4, b0); SBAR();
    mma(a1, b1); SBAR();
    loada(k4 + 1, a1); loadb(k4 + 1, b1); SBAR();
    mma(a2, b2); SBAR();
    loadb(48 + w, b2); SBAR();
    mma(a3, b3); SBAR();
    loadb(56 + w, b3); SBAR();
    mma(a0, b0); SBAR();
    loadb(64 + (w & 3), b0);
    u32x2 xv[3]; int xoff[3];
#pragma unroll
    for (int it = 0; it < 3; ++it) {
        const int tl = min(w + 8 * it, 19), i = tl / 5, n = tl - 5 * i;
        xoff[it] = (64 * rb + 16 * i + (lane & 15)) * 1280 + (80 * ct + 16 * n + 4 * (lane >> 4)) * 2;
        xv[it] = *(const GAS u32x2*)(x + xoff[it]);
    }
    SBAR();
    mma(a1, b1); SBAR();
    STAMPP(1, 8);
    flag_wait(ws, WS_FO + ((rb >> 2) * 32 + r32) * 128, (u32x2){~(unsigned)tag, 0u}, tag);
    STAMPP(1, 9);
    loada(48 + w, a2); loada(56 + w, a3); loada(64 + (w & 3), a0); SBAR();
    mma(a2, b2); mma(a3, b3);
    if (w < 4) mma(a0, b0);
    STAMPP(1, 10);
    lds_bar();
    LAS f32x4* red = (LAS f32x4*)lds;
#pragma unroll
    for (int i = 0; i < 4; ++i)
#pragma unroll
        for (int n = 0; n < 5; ++n) red[w * 1280 + (i * 5 + n) * 64 + lane] = acc[i][n];
    lds_bar();
#pragma unroll
    for (int it = 0; it < 3; ++it) {
        if (it < 2 || w < 4) {
            const int item = tid + 512 * it;
            f32x4 s = red[item];
#pragma unroll
            for (int k = 1; k < 8; ++k) s += red[k * 1280 + item];
            u32x2 v;
            v[0] = pk2(s[0] + __uint_as_float(xv[it][0] << 16), s[1] + __uint_as_float(xv[it][0] & 0xffff0000u));
            v[1] = pk2(s[2] + __uint_as_float(xv[it][1] << 16), s[3] + __uint_as_float(xv[it][1] & 0xffff0000u));
            *(GAS u32x2*)(out + xoff[it]) = v;
        }
    }
    STAMPP(1, 11);
    STAMP_OUT;
}

extern "C" void kernel_launch(void* const* d_in, const int* in_sizes, int n_in, void* d_out, int out_size, void* d_ws, size_t ws_size,
                              hipStream_t stream) {
    fused_block<<<256, 512, 0, stream>>>((const char*)d_in[0], (const char*)d_in[1], (const char*)d_in[2], (const char*)d_in[4],
                                         (const char*)d_in[5], (const char*)d_in[6], (char*)d_out, (char*)d_ws);
}
```

```cpp
#include <hip/hip_runtime.h>
#include <hip/hip_bf16.h>

#define STAMPS 0

typedef __bf16 bf16x8 __attribute__((ext_vector_type(8)));
typedef __bf16 bf16x2 __attribute__((ext_vector_type(2)));
typedef short s16x4 __attribute__((ext_vector_type(4)));
typedef float f32x32 __attribute__((ext_vector_type(32)));
typedef float f32x16 __attribute__((ext_vector_type(16)));
typedef float f32x4 __attribute__((ext_vector_type(4)));
typedef unsigned u32x32 __attribute__((ext_vector_type(32)));
typedef unsigned u32x16 __attribute__((ext_vector_type(16)));
typedef unsigned u32x8 __attribute__((ext_vector_type(8)));
typedef unsigned u32x4 __attribute__((ext_vector_type(4)));
typedef unsigned u32x2 __attribute__((ext_vector_type(2)));
typedef unsigned long long u64;
#define LAS __attribute__((address_space(3)))
#define GAS __attribute__((address_space(1)))
#define DEV __device__ __forceinline__

constexpr unsigned WS_EPOCH = 0;
constexpr unsigned WS_FA = 4096;
constexpr unsigned WS_FH = 8192;
constexpr unsigned WS_FO = 16384;
constexpr unsigned WS_GA = 65536;
constexpr unsigned WS_GB = WS_GA + 256 * 384 * 8;
constexpr unsigned WS_Q = WS_GB + 256 * 192 * 8;
constexpr unsigned WS_KV = WS_Q + 16 * 64 * 2560;
constexpr unsigned WS_HP = WS_KV + 16 * 64 * 5120;
constexpr unsigned WS_WP = WS_HP + 128 * 272 * 256;
constexpr unsigned WS_END = WS_WP + 40 * 68 * 1024;
constexpr unsigned WS_STAMP = 32u << 20;
constexpr unsigned OOB = 0x40000000u;

constexpr int L_TR = 131072;
constexpr int L_PART = L_TR + 16384;
constexpr int L_FIN = L_PART + 6144;
constexpr int L_FLAG = L_FIN + 5120;
constexpr int L_TOTAL = 163840;

constexpr float QSCALE = 0.22360679774997896f * 1.4426950408889634f;
constexpr int SC1 = 16;
constexpr unsigned SPIN_CAP = 1u << 22;

DEV unsigned pk2(float lo, float hi) { bf16x2 v = {(__bf16)lo, (__bf16)hi}; return __builtin_bit_cast(unsigned, v); }
DEV float bfr(float x) { return (float)(__bf16)x; }
DEV u32x2 split3(float v) {
    const float a = bfr(v), b = bfr(v - a), c = v - a - b;
    return (u32x2){pk2(a, b), pk2(c, 0.f)};
}
DEV float swap_add(float v) {
    auto r = __builtin_amdgcn_permlane32_swap(__float_as_uint(v), __float_as_uint(v), false, false);
    return __uint_as_float(r[0]) + __uint_as_float(r[1]);
}
DEV float swap_max(float v) {
    auto r = __builtin_amdgcn_permlane32_swap(__float_as_uint(v), __float_as_uint(v), false, false);
    return fmaxf(__uint_as_float(r[0]), __uint_as_float(r[1]));
}
DEV void glds16(unsigned voff, const void* sbase, unsigned ldsdst) {
    asm volatile("s_mov_b32 m0, %2\n\ts_nop 0\n\tglobal_load_lds_dwordx4 %0, %1" ::"v"(voff), "s"(sbase), "s"(ldsdst) : "memory");
}
DEV void glds4(unsigned voff, const void* sbase, unsigned ldsdst) {
    asm volatile("s_mov_b32 m0, %2\n\ts_nop 0\n\tglobal_load_lds_dword %0, %1" ::"v"(voff), "s"(sbase), "s"(ldsdst) : "memory");
}
template <int N> DEV void wait_bar() { asm volatile("s_waitcnt vmcnt(%0) lgkmcnt(0)\n\ts_barrier" ::"i"(N) : "memory"); }
DEV void lds_bar() { asm volatile("s_waitcnt lgkmcnt(0)\n\ts_barrier" ::: "memory"); }
DEV bf16x8 lds128(const LAS char* p) { return *(const LAS bf16x8*)p; }
DEV bf16x8 ldstr(const LAS char* p0, const LAS char* p1) {
    s16x4 a = __builtin_amdgcn_ds_read_tr16_b64_v4i16((LAS s16x4*)p0), b = __builtin_amdgcn_ds_read_tr16_b64_v4i16((LAS s16x4*)p1);
    return __builtin_bit_cast(bf16x8, __builtin_shufflevector(a, b, 0, 1, 2, 3, 4, 5, 6, 7));
}
DEV float dot_ones(bf16x8 v, float acc, unsigned ones = 0x3f803f80u) {
    const bf16x2 one = __builtin_bit_cast(bf16x2, ones);
#pragma unroll
    for (int i = 0; i < 4; ++i) acc = __builtin_amdgcn_fdot2_f32_bf16((bf16x2){v[2 * i], v[2 * i + 1]}, one, acc, false);
    return acc;
}
DEV float dot_self(bf16x8 v, float acc) {
#pragma unroll
    for (int i = 0; i < 4; ++i) { bf16x2 p = {v[2 * i], v[2 * i + 1]}; acc = __builtin_amdgcn_fdot2_f32_bf16(p, p, acc, false); }
    return acc;
}
#define MFMA32(a, b, c) __builtin_amdgcn_mfma_f32_32x32x16_bf16(a, b, c, 0, 0, 0)
#define MFMA8(a, b, c) __builtin_amdgcn_mfma_f32_32x32x8bf16_1k(__builtin_bit_cast(s16x4, a), __builtin_bit_cast(s16x4, b), c, 0, 0, 0)
#define MFMA16(a, b, c) __builtin_amdgcn_mfma_f32_16x16x32_bf16(a, b, c, 0, 0, 0)
#define SBAR() __builtin_amdgcn_sched_barrier(0)

#define STAMP(i) do {} while (0)
#define STAMPP(p, i) do {} while (0)
#define STAMP_DECL
#define STAMP_OUT do {} while (0)

DEV u32x2 ld2_async(const char* base, unsigned off) { u32x2 v; asm volatile("global_load_dwordx2 %0, %1, %2 sc1" : "=v"(v) : "v"(off), "s"(base) : "memory"); return v; }
DEV u32x4 ld4_async(const char* base, unsigned off) { u32x4 v; asm volatile("global_load_dwordx4 %0, %1, %2 sc1" : "=v"(v) : "v"(off), "s"(base) : "memory"); return v; }
DEV void flag_store(char* p, u64 tag) { __hip_atomic_store((GAS u64*)p, tag, __ATOMIC_RELAXED, __HIP_MEMORY_SCOPE_AGENT); }
DEV float gran_sum(const char* ws, unsigned off, unsigned stride, u32x2 (&g)[4], unsigned tag) {
#pragma unroll 1
    for (unsigned spin = 0; spin < SPIN_CAP; ++spin) {
        if (__all(g[0][1] == tag && g[1][1] == tag && g[2][1] == tag && g[3][1] == tag)) break;
        asm volatile("s_sleep 1\n\tglobal_load_dwordx2 %0, %4, %8 sc1\n\tglobal_load_dwordx2 %1, %5, %8 sc1\n\t"
                     "global_load_dwordx2 %2, %6, %8 sc1\n\tglobal_load_dwordx2 %3, %7, %8 sc1\n\ts_waitcnt vmcnt(0)"
                     : "=&v"(g[0]), "=&v"(g[1]), "=&v"(g[2]), "=&v"(g[3])
                     : "v"(off), "v"(off + stride), "v"(off + 2 * stride), "v"(off + 3 * stride), "s"(ws) : "memory");
    }
    return (__uint_as_float(g[0][0]) + __uint_as_float(g[1][0])) + (__uint_as_float(g[2][0]) + __uint_as_float(g[3][0]));
}
DEV void flag_wait(const char* ws, unsigned off, u32x2 f, u64 tag) {
#pragma unroll 1
    for (unsigned spin = 0; spin < SPIN_CAP; ++spin) {
        if (__all(f[0] == (unsigned)tag && f[1] == (unsigned)(tag >> 32))) break;
        asm volatile("s_sleep 1\n\tglobal_load_dwordx2 %0, %1, %2 sc1\n\ts_waitcnt vmcnt(0)" : "=&v"(f) : "v"(off), "s"(ws) : "memory");
    }
}
DEV void flag_wait4(const char* ws, unsigned off, u32x4 f, u32x4 g, u64 tag) {
    const unsigned lo = (unsigned)tag, hi = (unsigned)(tag >> 32);
#pragma unroll 1
    for (unsigned spin = 0; spin < SPIN_CAP; ++spin) {
        if (__all(f[0] == lo && f[1] == hi && f[2] == lo && f[3] == hi && g[0] == lo && g[1] == hi && g[2] == lo && g[3] == hi)) break;
        asm volatile("s_sleep 1\n\tglobal_load_dwordx4 %0, %2, %3 sc1\n\tglobal_load_dwordx4 %1, %2, %3 offset:1024 sc1\n\ts_waitcnt vmcnt(0)"
                     : "=&v"(f), "=&v"(g) : "v"(off), "s"(ws) : "memory");
    }
}
DEV void arrive_publish(LAS unsigned* cnt, int lane, char* flag, u64 tag) {
    if (lane == 0) {
        const unsigned old = __hip_atomic_fetch_add(cnt, 1u, __ATOMIC_RELAXED, __HIP_MEMORY_SCOPE_WORKGROUP);
        if (old == 7u) flag_store(flag, tag);
    }
}

#define EXP4(a, b, c, d) "v_exp_f32 v" #a ", v" #a "\n\tv_exp_f32 v" #b ", v" #b "\n\tv_exp_f32 v" #c ", v" #c "\n\tv_exp_f32 v" #d ", v" #d "\n\t"
#define E_S0_0 EXP4(64, 65, 66, 67)
#define E_S0_1 EXP4(68, 69, 70, 71)
#define E_S0_2 EXP4(72, 73, 74, 75)
#define E_S0_3 EXP4(76, 77, 78, 79)
#define E_S1_0 EXP4(80, 81, 82, 83)
#define E_S1_1 EXP4(84, 85, 86, 87)
#define E_S1_2 EXP4(88, 89, 90, 91)
#define E_S1_3 EXP4(92, 93, 94, 95)
#define E_S2_0 EXP4(96, 97, 98, 99)
#define E_S2_1 EXP4(100, 101, 102, 103)
#define E_S2_2 EXP4(104, 105, 106, 107)
#define E_S2_3 EXP4(108, 109, 110, 111)
#define E_S3_0 EXP4(112, 113, 114, 115)
#define E_S3_1 EXP4(116, 117, 118, 119)
#define E_S3_2 EXP4(120, 121, 122, 123)
#define E_S3_3 EXP4(124, 125, 126, 127)
#define R_S0 "v[64:79]"
#define R_S1 "v[80:95]"
#define R_S2 "v[96:111]"
#define R_S3 "v[112:127]"
#define R_S0L "v[64:71]"
#define R_S0H "v[72:79]"
#define R_S1L "v[80:87]"
#define R_S1H "v[88:95]"
#define R_S2L "v[96:103]"
#define R_S2H "v[104:111]"
#define R_S3L "v[112:119]"
#define R_S3H "v[120:127]"
#define R_O0 "v[128:143]"
#define R_O1 "v[144:159]"
#define R_V0A "v[160:163]"
#define R_V0B "v[164:167]"
#define R_V1A "v[168:171]"
#define R_V1B "v[172:175]"
#define R_V2A "v[176:179]"
#define R_V2B "v[180:183]"
#define R_V3A "v[184:187]"
#define R_V3B "v[188:191]"
#define R_K0M "v[192:195]"
#define R_K1M "v[196:199]"
#define R_K2M "v[200:203]"
#define R_K3M "v[204:207]"
#define R_K0R "v[208:209]"
#define R_K1R "v[210:211]"
#define R_K2R "v[212:213]"
#define R_K3R "v[214:215]"
#define R_Q0M "v[216:219]"
#define R_Q1M "v[220:223]"
#define R_Q0R "v[224:225]"
#define R_Q1R "v[226:227]"
#define QK1(s, k, q) "v_mfma_f32_32x32x16_bf16 " R_S##s ", " R_K##k##M ", " R_Q##q##M ", 0\n\t"
#define QK2(s, k, q) "v_mfma_f32_32x32x8_bf16 " R_S##s ", " R_K##k##R ", " R_Q##q##R ", " R_S##s "\n\t"
#define PV1(o, v, s) "v_smfmac_f32_32x32x32_bf16 " R_O##o ", " R_V##v##A ", " R_S##s##L ", v228\n\t"
#define PV2(o, v, s) "v_smfmac_f32_32x32x32_bf16 " R_O##o ", " R_V##v##B ", " R_S##s##H ", v228\n\t"
#define PERIOD(e, sn, k, q, sp, v, o) \
    QK1(sn, k, q) E_S##e##_0 PV1(o, v, sp) E_S##e##_1 QK2(sn, k, q) E_S##e##_2 PV2(o, v, sp) E_S##e##_3
#define LDK(k) \
    "global_load_dwordx4 " R_K##k##M ", v229, s[60:61] sc1\n\t" \
    "s_mov_b32 exec_hi, 0\n\t" \
    "global_load_dwordx2 " R_K##k##R ", v231, s[60:61] sc1\n\t"
#define LDV(v) \
    "s_mov_b64 exec, %[vmask]\n\t" \
    "global_load_dwordx4 " R_V##v##A ", v230, s[60:61] sc1\n\t" \
    "global_load_dwordx4 " R_V##v##B ", v230, s[60:61] offset:640 sc1\n\t"
#define ADV "s_mov_b64 exec, -1\n\ts_add_u32 s60, s60, 5120\n\ts_addc_u32 s61, s61, 0\n\t"
#define GROUP(n, k, v) "s_waitcnt vmcnt(" n ")\n\t" LDK(k) LDV(v) ADV
#define PERIOD_NOQK(e, sp, v, o) E_S##e##_0 PV1(o, v, sp) E_S##e##_1 E_S##e##_2 PV2(o, v, sp) E_S##e##_3
constexpr u64 VMASK = 0x000fffff000fffffull;

struct Attn { f32x32 sa, sb, oo; u32x32 vv; u32x16 km; u32x8 kr, qm; u32x4 qr, misc; const char* base; };
DEV void attn_prologue1(Attn& a, const char* kv, const char* q0, const char* q1) {
    a.base = kv - 2 * 5120;
    asm volatile(
        ".irp r,208,210,212,214\n\tv_mov_b32 v\\r, 0x3f803f80\n\t.endr\n\t"
        ".irp r,209,211,213,215\n\tv_mov_b32 v\\r, 0x3f80\n\t.endr\n\t"
        ".irp r,224,225,226,227\n\tv_mov_b32 v\\r, 0\n\t.endr\n\t"
        "global_load_dwordx4 " R_Q0M ", v229, %[q0] sc1\n\t"
        "global_load_dwordx4 " R_Q1M ", v229, %[q1] sc1\n\t"
        "s_mov_b32 exec_hi, 0\n\t"
        "global_load_dwordx2 " R_Q0R ", v231, %[q0] sc1\n\t"
        "global_load_dwordx2 " R_Q1R ", v231, %[q1] sc1\n\t"
        "s_mov_b64 exec, -1\n\t"
        LDK(0) ADV LDK(1) ADV
        : "=&{v[192:207]}"(a.km), "=&{v[208:215]}"(a.kr), "=&{v[216:223]}"(a.qm), "=&{v[224:227]}"(a.qr), "+{s[60:61]}"(a.base)
        : "{v[228:231]}"(a.misc), [q0] "s"(q0 - 10240), [q1] "s"(q1 - 10240)
        : "memory", "scc");
}
DEV void attn_init_v(Attn& a, unsigned vinit) {
    asm volatile(".irp r,160,161,162,163,164,165,166,167,168,169,170,171,172,173,174,175,176,177,178,179,180,181,182,183,184,185,186,187,188,189,190,191\n\tv_mov_b32 v\\r, %[vinit]\n\t.endr"
                 : "=&{v[160:191]}"(a.vv) : [vinit] "v"(vinit));
}
DEV void attn_init_o(Attn& a) {
    asm volatile(".irp r,128,129,130,131,132,133,134,135,136,137,138,139,140,141,142,143,144,145,146,147,148,149,150,151,152,153,154,155,156,157,158,159\n\tv_mov_b32 v\\r, 0\n\t.endr"
                 : "={v[128:159]}"(a.oo));
}
DEV void attn_init_s(Attn& a) {
    asm volatile(".irp r,112,113,114,115,116,117,118,119,120,121,122,123,124,125,126,127\n\tv_mov_b32 v\\r, 0\n\t.endr" : "={v[96:127]}"(a.sb));
}
DEV void attn_prologue2(Attn& a) {
    asm volatile(LDK(2) LDV(0) ADV LDK(3) LDV(1) ADV
                 : "+{v[160:191]}"(a.vv), "+{v[192:207]}"(a.km), "+{v[208:215]}"(a.kr), "+{s[60:61]}"(a.base)
                 : "{v[228:231]}"(a.misc), [vmask] "s"(VMASK) : "memory", "scc");
}
template <int N> DEV void attn_first_scores(Attn& a) {
    asm volatile("s_waitcnt vmcnt(%[n])\n\ts_nop 1\n\t" QK1(0, 0, 0) QK1(1, 0, 1) QK2(0, 0, 0) QK2(1, 0, 1) "s_nop 11\n\t"
                 : "=&{v[64:95]}"(a.sa), "+{v[192:207]}"(a.km), "+{v[208:215]}"(a.kr), "+{v[216:223]}"(a.qm), "+{v[224:227]}"(a.qr)
                 : [n] "i"(N) : "memory");
}
#define ITER(n0, n1, n2, n3, e0, e1, e2, e3) \
        GROUP(n0, 0, 2) e0 PERIOD(0, 2, 1, 0, 3, 3, 1) PERIOD(1, 3, 1, 1, 0, 0, 0) \
        GROUP(n1, 1, 3) e1 PERIOD(2, 0, 2, 0, 1, 0, 1) PERIOD(3, 1, 2, 1, 2, 1, 0) \
        GROUP(n2, 2, 0) e2 PERIOD(0, 2, 3, 0, 3, 1, 1) PERIOD(1, 3, 3, 1, 0, 2, 0) \
        GROUP(n3, 3, 1) e3 PERIOD(2, 0, 0, 0, 1, 2, 1) PERIOD(3, 1, 0, 1, 2, 3, 0)
#define ATTN_STATE(a) "+{v[64:95]}"(a.sa), "+{v[96:127]}"(a.sb), "+{v[128:159]}"(a.oo), "+{v[160:191]}"(a.vv), "+{v[192:207]}"(a.km), "+{v[208:215]}"(a.kr), "+{s[60:61]}"(a.base)
#define ATTN_IN(a) "{v[216:223]}"(a.qm), "{v[224:227]}"(a.qr), "{v[228:231]}"(a.misc), [vmask] "s"(VMASK)
template <int X> DEV void attn_iter(Attn& a) {
    asm volatile(ITER("%[n]", "%[n]", "4", "4", , , , ) : ATTN_STATE(a) : ATTN_IN(a), [n] "i"(4 + X) : "memory", "scc");
}
#define PF1(r, p, o) "global_load_dwordx4 " r ", %[v], " p o " sc1\n\t"
#define PF2(r0, r1, p) PF1(r0, p, "") PF1(r1, p, " offset:1024")
#define PFA "s_add_u32 s62, s62, 69632\n\ts_addc_u32 s63, s63, 0\n\t"
#define PFB "s_add_u32 s64, s64, 69632\n\ts_addc_u32 s65, s65, 0\n\t"
DEV void attn_last(Attn& a, u32x32& pfa, u32x32& pfb, u32x8& pfc, const char* pa, const char* pb, unsigned voff) {
    asm volatile(
        ITER("4", "5", "6", "6", PF1("v[0:3]", "s[62:63]", ""), PF1("v[16:19]", "s[62:63]", " offset:1024") PFA, PF1("v[4:7]", "s[62:63]", ""), PF1("v[20:23]", "s[62:63]", " offset:1024") PFA)
        "s_setprio 1\n\t"
        ITER("6", "6", "6", "6", PF1("v[8:11]", "s[62:63]", ""), PF1("v[24:27]", "s[62:63]", " offset:1024") PFA, PF1("v[12:15]", "s[62:63]", ""), PF1("v[28:31]", "s[62:63]", " offset:1024"))
        "s_waitcnt vmcnt(6)\n\t" LDV(2) ADV PF2("v[32:35]", "v[48:51]", "s[64:65]") PFB
        PERIOD(0, 2, 1, 0, 3, 3, 1) LDV(3) ADV
        PF2("v[36:39]", "v[52:55]", "s[64:65]") PFB
        PERIOD(1, 3, 1, 1, 0, 0, 0)
        "s_waitcnt vmcnt(9)\n\t"
        PF2("v[40:43]", "v[56:59]", "s[64:65]") PFB
        PERIOD(2, 0, 2, 0, 1, 0, 1)
        PF2("v[44:47]", "v[60:63]", "s[64:65]") PFB
        PERIOD(3, 1, 2, 1, 2, 1, 0)
        "s_setprio 0\n\ts_waitcnt vmcnt(6)\n\t"
        PF2("v[232:235]", "v[236:239]", "s[64:65]")
        PERIOD(0, 2, 3, 0, 3, 1, 1)
        PERIOD(1, 3, 3, 1, 0, 2, 0)
        PERIOD_NOQK(2, 1, 2, 1) PERIOD_NOQK(3, 2, 3, 0)
        "s_nop 1\n\t" PV1(1, 3, 3) PV2(1, 3, 3) "s_nop 15\n\t"
        : ATTN_STATE(a), "=&{v[0:31]}"(pfa), "=&{v[32:63]}"(pfb), "=&{v[232:239]}"(pfc), "+{s[62:63]}"(pa), "+{s[64:65]}"(pb)
        : ATTN_IN(a), [v] "v"(voff)
        : "memory", "scc");
}
template <int O, class V> DEV float max16(const V& s) {
    float a = fmaxf(fmaxf(s[O], s[O + 1]), s[O + 2]), b = fmaxf(fmaxf(s[O + 3], s[O + 4]), s[O + 5]);
    a = fmaxf(fmaxf(a, s[O + 6]), s[O + 7]); b = fmaxf(fmaxf(b, s[O + 8]), s[O + 9]);
    a = fmaxf(fmaxf(a, s[O + 10]), s[O + 11]); b = fmaxf(fmaxf(b, s[O + 12]), s[O + 13]);
    a = fmaxf(fmaxf(a, s[O + 14]), s[O + 15]);
    return fmaxf(a, b);
}
DEV void attn_set_reference(Attn& a, float (&c)[2], int hh) {
#pragma unroll
    for (int i = 0; i < 2; ++i) {
        c[i] = swap_max(c[i]);
        const u32x2 t = split3(-c[i]);
        if (hh) { a.qr[2 * i] = t[0]; a.qr[2 * i + 1] = t[1]; }
    }
}

struct Ring {
    const char *x, *wq, *wgu;
    unsigned xo0, xo1, wo0, wo1, go0, go1, go2;
    unsigned lds0; int w, s0;
};
template <int C, int P> DEV void ring_piece(const Ring& r) {
    if constexpr (C < 20) {
        int cc = r.s0 + C % 10; cc = cc >= 10 ? cc - 10 : cc;
        if constexpr (C < 10) {
            const unsigned d = r.lds0 + (C & 3) * 32768 + r.w * 2048;
            if constexpr (P == 0) glds16(r.xo0, r.x + cc * 128, d);
            if constexpr (P == 1) glds16(r.xo1, r.x + cc * 128, d + 1024);
            if constexpr (P == 2) glds16(r.wo0, r.wq + cc * (64 * 3840), d + 16384);
            if constexpr (P == 3) glds16(r.wo1, r.wq + cc * (64 * 3840), d + 16384 + 1024);
        } else {
            const unsigned d = r.lds0 + (C & 3) * 32768 + r.w * 3072 + (r.w >> 2) * 32;
            const char* b = r.wgu + cc * (64 * 3072);
            if constexpr (P == 0) glds16(r.go0, b, d);
            if constexpr (P == 1) glds16(r.go1, b, d + 1024);
            if constexpr (P == 2) glds16(r.go2, b, d + 2048);
        }
    }
}
template <int C> DEV void ring_issue(const Ring& r) { ring_piece<C, 0>(r); ring_piece<C, 1>(r); ring_piece<C, 2>(r); ring_piece<C, 3>(r); }
constexpr int ring_n(int c) { return c < 10 ? 4 : (c < 20 ? 3 : 0); }
constexpr int vm_pre(int c) { return c == 6 ? 4 : (c == 15 ? 5 : 0); }
constexpr int vm_post(int c) { return (c >= 0 && c <= 4 ? 1 : 0) + ((c == 2 || c == 11) ? 1 : 0) + ((c == 9 || c == 12 || c == 16) ? 4 : 0) + (c == 17 ? 8 : 0); }
constexpr int vm_wait(int c) {
    if (c == 11) return ring_n(14);
    return vm_post(c - 3) + vm_pre(c - 2) + ring_n(c + 2) + vm_post(c - 2) + vm_pre(c - 1) + ring_n(c + 3) + vm_post(c - 1);
}

__global__ __launch_bounds__(512, 2) void fused_block(const char* x, const char* wqkv, const char* wo, const char* wg, const char* wu, const char* wd,
                                                      char* out, char* ws) {
    __shared__ __attribute__((aligned(16))) char smem[L_TOTAL];
    LAS char* lds = (LAS char*)smem;
    const int w = __builtin_amdgcn_readfirstlane(threadIdx.x >> 6);
    const int bid = blockIdx.x, xcd = bid & 7, rr = bid >> 3;
#define LANE_IDS int lane = __builtin_amdgcn_mbcnt_hi(~0u, __builtin_amdgcn_mbcnt_lo(~0u, 0u)); asm volatile("" : "+v"(lane)); \
    const int tid = 64 * w + lane, r32 = lane & 31, hh = lane >> 5; (void)tid; (void)r32; (void)hh
    asm volatile("" : "+s"(x), "+s"(wqkv), "+s"(wo), "+s"(wg), "+s"(wu), "+s"(wd), "+s"(out), "+s"(ws));
    const __amdgpu_buffer_rsrc_t wsr = __builtin_amdgcn_make_buffer_rsrc(ws, 0, WS_END, 0x00020000);
    LAS unsigned* lflag = (LAS unsigned*)(lds + L_FLAG);
    u64 tag;
    STAMP_DECL;
    STAMP(0);
    const int mi = rr & 7, ji = rr >> 3, m = 8 * (xcd & 1) + mi, j = 4 * (xcd >> 1) + ji;
    const int wm = w & 3, wn = w >> 2;
    const int g = 4 * xcd + (rr >> 3), mq = rr & 7, qq = w & 3, kh = w >> 2, odd = g & 1;
    const char* qp = ws + WS_Q + ((g >> 1) * 64 + 8 * mq + 2 * qq) * 2560;
    const unsigned kvoff = WS_KV + ((g >> 1) * 64 + 32 * kh) * 5120;
    Attn at;
    float c[2];
    {
        LANE_IDS;
        Ring rg;
        rg.x = x + m * 128 * 1280; rg.wq = wqkv + (j < 15 ? j : 14) * 256; rg.wgu = (w < 4 ? wg : wu) + j * 192;
        rg.lds0 = (unsigned)(size_t)lds; rg.w = w;
        rg.s0 = (((5 * ji + 1) >> 1) + mi + (mi >> 2)) % 10;
        rg.xo0 = (16 * w + (lane >> 3)) * 1280 + (((lane & 7) ^ (lane >> 4)) << 4);
        rg.xo1 = (16 * w + 8 + (lane >> 3)) * 1280 + (((lane & 7) ^ (4 + (lane >> 4))) << 4);
        rg.wo0 = (8 * w + (lane >> 4)) * 3840 + (((lane & 15) ^ ((lane >> 4) << 2)) << 4);
        rg.wo1 = rg.wo0 + 4 * 3840;
        ring_issue<0>(rg); __builtin_amdgcn_s_barrier();
        ring_issue<1>(rg);
        asm volatile("s_load_dwordx2 %0, %1, 0x0\n\ts_waitcnt lgkmcnt(0)" : "=s"(tag) : "s"(ws) : "memory");
        tag += 1;
        const unsigned tag32 = (unsigned)tag;
        { const int p0 = (w & 3) * 192 + lane, p1 = p0 + 64, p2 = p0 + 128;
          rg.go0 = (p0 / 12) * 3072 + (p0 % 12) * 16; rg.go1 = (p1 / 12) * 3072 + (p1 % 12) * 16; rg.go2 = (p2 / 12) * 3072 + (p2 % 12) * 16; }
        if (tid < 4) lflag[tid] = 0u;

        const int g4 = lane >> 4, q4 = (lane >> 2) & 3, p4 = lane & 3, rot = wm ^ (2 * wn);
        const int xrow = 32 * wm + r32;
        const unsigned xa = (xrow * 128 + ((hh ^ ((xrow >> 1) & 7)) << 4)) ^ (rot << 5);
        const unsigned wab = 16384 + (rot << 12) + (8 * (g4 >> 1) + q4) * 256 + (g4 & 1) * 32 + (p4 & 1) * 16 + (p4 >> 1) * 8;
        const unsigned wa0 = wab + (((2 * wn) ^ q4) << 6), wa1 = wab + (((2 * wn + 1) ^ q4) << 6);
        LAS float* part = (LAS float*)(lds + L_PART); LAS u32x2* fin = (LAS u32x2*)(lds + L_FIN);
        bf16x8 xf[40], fw[2][3];
        f32x16 acc[2] = {};
        float rs = 0.f, rq = 0.f, cs0 = 0.f, cs1 = 0.f, pv[4];
        u32x2 gr[4], fa;
        const unsigned col2 = (mi & 3) == 3 ? 0x3f803f80u : 0u;
        int pw_row = (2 * wn + hh) * 384 + xrow, pw_col = wm * 384 + 256 + 64 * wn + lane, pr_i = w < 6 ? tid : lane;
        unsigned pub_o = tid * 8 + (w < 6 ? WS_GA + bid * 3072 : OOB), warm_o = min(lane, 47) * 16;
        asm volatile("" : "+v"(pw_row), "+v"(pw_col), "+v"(pr_i), "+v"(pub_o), "+v"(warm_o), "+v"(rg.go0), "+v"(rg.go1), "+v"(rg.go2));
        const char* warm_p = rg.wgu - ji * 192 + (4 * rr + wm) * (5 * 3072);
        const unsigned gab = w < 4 ? WS_GA + (xcd + 8 * mi) * 3072 + w * 512 : WS_GA + (xcd + 64 * ji + 32 * ((w >> 1) & 1)) * 3072 + 2048 + (w & 1) * 512;
        const unsigned gao = gab + lane * 8, gas = w < 4 ? 64 * 3072 : 8 * 3072;
#define RW1(f, b, c, k) fw[f][b] = ldstr(lds + ((c) & 3) * 32768 + ((b ? wa1 : wa0) ^ ((k) << 12)), lds + ((c) & 3) * 32768 + ((b ? wa1 : wa0) ^ ((k) << 12)) + 1024)
#define RX1(c, k) xf[4 * (c) + (k)] = lds128(lds + ((c) & 3) * 32768 + (xa ^ ((k) << 5)))
#define M1(f, b, c, k) acc[b] = MFMA32(fw[f][b], xf[4 * (c) + (k)], acc[b]); SBAR()
        STAMPP(2, 1);
        wait_bar<4>();
        STAMPP(2, 2);
        RW1(0, 0, 0, 0); RX1(0, 0); RW1(0, 1, 0, 0); RW1(1, 0, 0, 1); RX1(0, 1); RW1(1, 1, 0, 1); SBAR();
        ring_issue<2>(rg); ring_piece<3, 0>(rg); SBAR();
#define A1C(c, H3, H4, H5, H6) do { constexpr int n_ = c < 9 ? c + 1 : 0; \
        M1(0, 0, c, 0); RW1(0, 0, c, 2); RX1(c, 2); ring_piece<c + 3, 1>(rg); SBAR(); \
        M1(0, 1, c, 0); RW1(0, 1, c, 2); ring_piece<c + 3, 2>(rg); SBAR(); \
        M1(1, 0, c, 1); RW1(1, 0, c, 3); RX1(c, 3); ring_piece<c + 3, 3>(rg); SBAR(); \
        M1(1, 1, c, 1); RW1(1, 1, c, 3); H3; SBAR(); \
        M1(0, 0, c, 2); H4; SBAR(); M1(0, 1, c, 2); H5; SBAR(); \
        wait_bar<vm_wait(c)>(); \
        M1(1, 0, c, 3); \
        if constexpr (c < 9) { RW1(0, 0, n_, 0); RX1(n_, 0); RW1(0, 1, n_, 0); RW1(1, 0, n_, 1); RX1(n_, 1); } \
        H6; SBAR(); \
        M1(1, 1, c, 3); \
        if constexpr (c < 9) { RW1(1, 1, n_, 1); ring_piece<c + 4, 0>(rg); } else ring_issue<c + 4>(rg); \
        SBAR(); } while (0)
#define WARM(i) glds16(warm_o, warm_p + (i) * 3072, rg.lds0 + L_TR + w * 2048)
        A1C(0, , , ({ rs = dot_ones(xf[0], rs); rq = dot_self(xf[0], rq); rs = dot_ones(xf[1], rs); rq = dot_self(xf[1], rq);
                      cs0 = dot_ones(fw[0][0], cs0); cs1 = dot_ones(fw[0][1], cs1); }), );
        STAMPP(2, 3);
        A1C(1, WARM(0), , ({ rs = dot_ones(xf[4], rs); rq = dot_self(xf[4], rq); rs = dot_ones(xf[5], rs); rq = dot_self(xf[5], rq);
                      cs0 = dot_ones(fw[0][0], cs0, col2); cs1 = dot_ones(fw[0][1], cs1, col2); }), );
        STAMPP(2, 4);
        A1C(2, WARM(1), ({ cs0 = swap_add(cs0); cs1 = swap_add(cs1); part[pw_col] = hh ? cs1 : cs0; }),
            ({ float r2 = dot_ones(xf[8], rs), q2 = dot_self(xf[8], rq); r2 = dot_ones(xf[9], r2); q2 = dot_self(xf[9], q2);
               part[pw_row] = (ji & 1) ? rs : r2; part[pw_row + 128] = (ji & 1) ? rq : q2; }),
            ({ pv[0] = part[pr_i]; pv[1] = part[384 + pr_i]; pv[2] = part[768 + pr_i]; pv[3] = part[1152 + pr_i]; }));
        STAMPP(2, 5);
        A1C(3, WARM(2), __builtin_amdgcn_raw_buffer_store_b64((u32x2){__float_as_uint((pv[0] + pv[1]) + (pv[2] + pv[3])), tag32}, wsr, pub_o, 0, SC1), , );
        STAMPP(2, 6); A1C(4, WARM(3), , , ); STAMPP(2, 7); A1C(5, WARM(4), , , ); STAMPP(2, 8);
        A1C(6, , , , ({ gr[0] = ld2_async(ws, gao); gr[1] = ld2_async(ws, gao + gas); gr[2] = ld2_async(ws, gao + 2 * gas); gr[3] = ld2_async(ws, gao + 3 * gas); }));
        STAMPP(2, 9); A1C(7, , , , ); STAMPP(2, 10);
        float rstd, st0, st1;
        u32x2 nmu, cf[3];
        A1C(8, ({ asm volatile("s_waitcnt vmcnt(6)" : "+v"(gr[0]), "+v"(gr[1]), "+v"(gr[2]), "+v"(gr[3]) :: "memory");
                  st0 = gran_sum(ws, gao, gas, gr, tag32); }),
            ({ u32x2 t = split3(st0); if (w < 4) t[0] = __float_as_uint(st0);
               fin[(w < 6 ? 64 * w : 64 * w + 64) + lane] = t; }), ,
            ({ st0 = *(const LAS float*)(fin + xrow); st1 = *(const LAS float*)(fin + 128 + xrow);
               cf[0] = fin[256 + 192 * hh + 64 * wn + r32]; cf[1] = fin[256 + 192 * hh + 64 * wn + 32 + r32]; }));
        A1C(9, ({ const float mean = st0 * (1.f / 640.f), var = fmaxf(st1 * (1.f / 640.f) - mean * mean, 0.f);
                  nmu = (u32x2){pk2(-mean, -mean), pk2(-mean, 0.f)};
                  rstd = __builtin_amdgcn_rcpf(bfr(__builtin_amdgcn_sqrtf(bfr(bfr(var) + 1e-5f))));
                  asm volatile("" : "+v"(nmu), "+v"(rstd)); }),
            , ({ acc[0] = MFMA8(cf[0], nmu, acc[0]); acc[1] = MFMA8(cf[1], nmu, acc[1]); }), );
        STAMPP(2, 11);
        STAMPP(1, 1);
        { const int tile = 4 * m + wm;
          const float sc = j < 5 ? rstd * QSCALE : rstd;
          u32x4 y[4];
#pragma unroll
          for (int i = 0; i < 4; ++i)
#pragma unroll
              for (int e = 0; e < 4; ++e) y[i][e] = pk2(sc * acc[i >> 1][8 * (i & 1) + 2 * e], sc * acc[i >> 1][8 * (i & 1) + 2 * e + 1]);
          if (j < 10) {
              const unsigned base = (j < 5 ? WS_Q + tile * 2560 : WS_KV + tile * 5120) + r32 * 16, ps = j < 5 ? 64 * 2560 : 64 * 5120;
              const int c8 = (j < 5 ? j : j - 5) * 16 + 8 * wn + hh;
#pragma unroll
              for (int i = 0; i < 4; ++i) {
                  const int ci = c8 + 2 * i, pr = ci / 5, pl = ci - 5 * pr;
                  __builtin_amdgcn_raw_buffer_store_b128(y[i], wsr, base + pr * ps + pl * 512, 0, SC1);
              }
          } else {
              LAS char* tp = lds + L_TR + w * 2048;
              const int cu = 2 * (g4 & 1) + (p4 >> 1);
#pragma unroll
              for (int b = 0; b < 2; ++b) {
#pragma unroll
                  for (int v = 0; v < 2; ++v) *(LAS u32x4*)(tp + r32 * 64 + (((2 * v + hh) ^ ((r32 >> 1) & 3)) << 4)) = y[2 * b + v];
                  asm volatile("s_waitcnt lgkmcnt(0)" ::: "memory");
                  const int col = (j - 10) * 128 + 64 * wn + 32 * b + r32, head = col / 20, d = col - 20 * head;
                  const unsigned base = j < 15 ? WS_KV + ((head >> 1) * 64 + tile) * 5120 + 2560 + (head & 1) * 1280 + d * 16 + hh * 320 : OOB;
#pragma unroll
                  for (int i = 0; i < 2; ++i) {
                      const int r0 = 16 * i + 8 * hh + q4;
                      const bf16x8 v = ldstr(tp + r0 * 64 + ((cu ^ (q4 >> 1)) << 4) + (p4 & 1) * 8,
                                             tp + (r0 + 4) * 64 + ((cu ^ (2 + (q4 >> 1))) << 4) + (p4 & 1) * 8);
                      __builtin_amdgcn_raw_buffer_store_b128(__builtin_bit_cast(u32x4, v), wsr, base + i * 640, 0, SC1);
                  }
                  asm volatile("s_waitcnt lgkmcnt(0)" ::: "memory");
              }
          } }
        STAMPP(1, 2); STAMPP(4, 1);
        unsigned ga[4];
#pragma unroll
        for (int ks = 0; ks < 4; ++ks) ga[ks] = (g4 & 1) * (12288 + 32) + (8 * (g4 >> 1) + q4) * 192 + wn * 96 + p4 * 8 + (ks ^ rot) * 3072;
        float cs[3] = {0.f, 0.f, 0.f};
        f32x16 ac2[3] = {};
#define RW2(f, b, c, k) fw[f][b] = ldstr(lds + ((c) & 3) * 32768 + ga[k] + b * 32, lds + ((c) & 3) * 32768 + ga[k] + b * 32 + 768)
#define M2(f, b, c, k) ac2[b] = MFMA32(fw[f][b], xf[4 * ((c) - 10) + (k)], ac2[b]); SBAR()
        SBAR(); RW2(0, 0, 10, 0); RW2(0, 1, 10, 0); RW2(0, 2, 10, 0); RW2(1, 0, 10, 1); RW2(1, 1, 10, 1); RW2(1, 2, 10, 1); SBAR();
#define A2C(c, H0, H1, H2, H3, H5, H6) do { \
        M2(0, 0, c, 0); RW2(0, 0, c, 2); H0; SBAR(); \
        M2(0, 1, c, 0); RW2(0, 1, c, 2); H1; SBAR(); \
        M2(0, 2, c, 0); RW2(0, 2, c, 2); H2; SBAR(); \
        M2(1, 0, c, 1); RW2(1, 0, c, 3); H3; SBAR(); \
        M2(1, 1, c, 1); RW2(1, 1, c, 3); SBAR(); \
        M2(1, 2, c, 1); RW2(1, 2, c, 3); SBAR(); \
        M2(0, 0, c, 2); M2(0, 1, c, 2); H5; SBAR(); \
        if constexpr (c < 19) wait_bar<vm_wait(c)>(); \
        M2(0, 2, c, 2); \
        if constexpr (c < 19) { RW2(0, 0, c + 1, 0); RW2(0, 1, c + 1, 0); RW2(0, 2, c + 1, 0); } \
        H6; SBAR(); \
        M2(1, 0, c, 3); if constexpr (c < 19) RW2(1, 0, c + 1, 1); ring_piece<c + 4, 0>(rg); SBAR(); \
        M2(1, 1, c, 3); if constexpr (c < 19) RW2(1, 1, c + 1, 1); ring_piece<c + 4, 1>(rg); SBAR(); \
        M2(1, 2, c, 3); if constexpr (c < 19) RW2(1, 2, c + 1, 1); ring_piece<c + 4, 2>(rg); SBAR(); STAMPP(4, c - 8); } while (0)
        A2C(10, , , , , ({ cs[0] = dot_ones(fw[0][0], cs[0]); cs[1] = dot_ones(fw[0][1], cs[1]); cs[2] = dot_ones(fw[0][2], cs[2]); }), );
        A2C(11, , , , , ({ cs[0] = dot_ones(fw[0][0], cs[0], col2); cs[1] = dot_ones(fw[0][1], cs[1], col2); cs[2] = dot_ones(fw[0][2], cs[2], col2);
                           cs[0] = swap_add(cs[0]); cs[1] = swap_add(cs[1]); cs[2] = swap_add(cs[2]);
                           part[wm * 384 + 96 * wn + lane] = hh ? cs[1] : cs[0]; part[wm * 384 + 96 * wn + 64 + r32] = cs[2]; }),
            ({ const int pi = w < 3 ? tid : lane; pv[0] = part[pi]; pv[1] = part[384 + pi]; pv[2] = part[768 + pi]; pv[3] = part[1152 + pi]; }));
        if (tid == 0) flag_store(ws + WS_FA + bid * 8, tag);
        A2C(12, __builtin_amdgcn_raw_buffer_store_b64((u32x2){__float_as_uint((pv[0] + pv[1]) + (pv[2] + pv[3])), tag32}, wsr, tid * 8 + (w < 3 ? WS_GB + bid * 1536 : OOB), 0, SC1), , , , , );
        const int unit = bid + 256 * w, us = unit / 10, nb = unit - 10 * us;
        const char* psrc = us < 48 ? wd : wo;
        const u32x4 psrd = {(unsigned)(size_t)psrc, (unsigned)((size_t)psrc >> 32) & 0xffffu, unit < 680 ? (us < 48 ? 1536u * 1280u : 640u * 1280u) : 0u, 0x00020000u};
        const unsigned plds = L_TR + min(w, 3) * 4096, pvo = (hh * (us < 64 ? 8 : 20) + ((lane >> 3) & 3)) * 1280 + (lane & 7) * 16;
#define PLD(i) ({ const int kap = 4 * us + i, r0 = us < 48 ? 16 * (kap >> 1) + 4 * (kap & 1) : (us < 64 ? 20 * ((kap >> 1) - 96) + 4 * (kap & 1) : 40 * (kap - 256) + 16); \
            asm volatile("s_mov_b32 m0, %3\n\ts_nop 0\n\tbuffer_load_dwordx4 %0, %1, %2 offen lds" :: "v"(pvo), "s"(psrd), "s"(r0 * 1280 + nb * 128), "s"(rg.lds0 + plds + i * 1024) : "memory"); })
        A2C(13, PLD(0), PLD(1), PLD(2), PLD(3), , );
        A2C(14, , , , , , );
        const int gbh = w >= 3 && w < 6;
        const unsigned gbo = WS_GB + (xcd + 64 * ji + 32 * gbh) * 1536 + (w < 6 ? w - 3 * gbh : 0) * 512 + lane * 8, fao = WS_FA + (tid & 255) * 8;
        A2C(15, , , , , , ({ gr[0] = ld2_async(ws, gbo); gr[1] = ld2_async(ws, gbo + 8 * 1536); gr[2] = ld2_async(ws, gbo + 16 * 1536); gr[3] = ld2_async(ws, gbo + 24 * 1536);
                   fa = ld2_async(ws, fao); }));
        A2C(16, , , , , , );
        const unsigned pdst = lane * 16 + (unit < 680 ? WS_WP + (4 * nb * 68 + us) * 1024 : OOB);
        const LAS char* prd = lds + plds + (8 * g4 + q4) * 128 + p4 * 8;
        bf16x8 pk[4];
#define PST(i) __builtin_amdgcn_raw_buffer_store_b128(__builtin_bit_cast(u32x4, pk[i]), wsr, pdst + i * (68 * 1024), 0, SC1)
        A2C(17, ({ asm volatile("s_waitcnt vmcnt(3)" : "+v"(gr[0]), "+v"(gr[1]), "+v"(gr[2]), "+v"(gr[3]), "+v"(fa) :: "memory");
                   st0 = gran_sum(ws, gbo, 8 * 1536, gr, tag32);
                   flag_wait(ws, fao, fa, tag); }),
            ({ fin[256 + (w < 6 ? tid : lane)] = split3(st0);
               pk[0] = ldstr(prd, prd + 512); pk[1] = ldstr(prd + 32, prd + 544); }),
            ({ pk[2] = ldstr(prd + 64, prd + 576); pk[3] = ldstr(prd + 96, prd + 608); }), ,
            ({ PST(0); PST(1); PST(2); PST(3); }),
            ({ cf[0] = fin[256 + 192 * hh + 96 * wn + r32]; cf[1] = fin[256 + 192 * hh + 96 * wn + 32 + r32]; cf[2] = fin[256 + 192 * hh + 96 * wn + 64 + r32]; }));
        A2C(18, at.misc = ((u32x4){0xddddu, 10240u + odd * 1536 + lane * 16, 2560u + odd * 1280 + hh * 320 + r32 * 16, 10240u + 1024 + r32 * 16 + odd * 8}),
            attn_prologue1(at, ws + kvoff, qp + kh * 2560, qp + (kh ^ 1) * 2560), ,
            ({ ac2[0] = MFMA8(cf[0], nmu, ac2[0]); ac2[1] = MFMA8(cf[1], nmu, ac2[1]); ac2[2] = MFMA8(cf[2], nmu, ac2[2]); }), , );
        A2C(19, attn_init_v(at, r32 == 20 ? 0x3f803f80u : 0u),
            attn_prologue2(at), attn_init_o(at), attn_init_s(at), , );
        STAMPP(1, 3);
        __builtin_amdgcn_s_setprio(3);
        attn_first_scores<10>(at);
        { const unsigned base = WS_HP + (8 * m + 2 * wm + (r32 >> 4)) * (272 * 256) + (2 * (6 * j + 3 * wn) + hh) * 256 + (r32 & 15) * 16;
          const float r2 = rstd * rstd;
#pragma unroll
          for (int b = 0; b < 3; ++b) {
              u32x4 v;
#pragma unroll
              for (int e = 0; e < 4; ++e) v[e] = pk2(ac2[b][2 * e] * r2 * ac2[b][8 + 2 * e], ac2[b][2 * e + 1] * r2 * ac2[b][9 + 2 * e]);
              __builtin_amdgcn_raw_buffer_store_b128(v, wsr, base + b * 512, 0, SC1);
          } }
        STAMPP(1, 4); STAMPP(3, 1);
        c[0] = max16<0>(at.sa); c[1] = max16<16>(at.sa);
    }
    const int rb = 4 * xcd + (rr >> 3), ct = rr & 7;
    const unsigned aob = WS_HP + 4 * rb * (272 * 256), bob = WS_WP + 5 * ct * (68 * 1024);
    const int krot = 2 * (((rr >> 3) + ct) % 3), k2 = 6 * w + (krot + 2) % 6, k4 = 6 * w + (krot + 4) % 6;
    u32x32 pfa, pfb; u32x8 pfc;
#pragma unroll 1
    for (int attempt = 0;; ++attempt) {
        LANE_IDS;
        attn_set_reference(at, c, hh);
        asm volatile("" : "+v"(c[0]), "+v"(c[1]));
        attn_first_scores<63>(at);
        at.base = ws + kvoff + 2 * 5120;
        STAMPP(1, 5); STAMPP(3, 2);
        attn_iter<3>(at); STAMPP(3, 3);
        arrive_publish(lflag + 1, lane, ws + WS_FH + bid * 8, tag);
        __builtin_amdgcn_s_setprio(2);
        attn_iter<0>(at); STAMPP(3, 4);
        attn_iter<0>(at); STAMPP(3, 5);
        u32x4 fh0 = ld4_async(ws, WS_FH + lane * 16), fh1 = ld4_async(ws, WS_FH + 1024 + lane * 16);
        attn_iter<2>(at); STAMPP(3, 6);
        asm volatile("" : "+v"(fh0), "+v"(fh1));
        flag_wait4(ws, WS_FH + lane * 16, fh0, fh1, tag);
        if (bid == 0 && tid == 0) flag_store(ws + WS_EPOCH, tag);
        { const unsigned li = (rr * 8 + w) * 25 + min(lane, 24), lt = li / 160;
          glds4(WS_WP + lt * (68 * 1024) + 48 * 1024 + (li - 160 * lt) * 128, ws, (unsigned)(size_t)lds + L_TR + w * 256); }
        STAMPP(3, 7);
        attn_iter<1>(at); STAMPP(3, 8); STAMPP(3, 9);
        attn_last(at, pfa, pfb, pfc, ws + aob + (6 * w + krot) * 1024, ws + bob + (6 * w + krot) * 1024, lane * 16);
        STAMPP(1, 6); STAMPP(3, 10);
        { int ln = __builtin_amdgcn_mbcnt_hi(~0u, __builtin_amdgcn_mbcnt_lo(~0u, 0u)); asm volatile("" : "+v"(ln));
          LAS float* snd = (LAS float*)lds + w * 13 * 64 + ln;
          float chk0 = (at.oo[8] * 0x1p30f) * 0.f, chk1 = (at.oo[24] * 0x1p30f) * 0.f;
#pragma unroll
          for (int i = 0; i < 12; ++i) { chk0 = __builtin_fmaf(at.oo[i], 0.f, chk0); chk1 = __builtin_fmaf(at.oo[16 + i], 0.f, chk1); }
          if (!(chk0 + chk1 == 0.f)) lflag[0] = 1u;
#pragma unroll
          for (int i = 0; i < 12; ++i) snd[i * 64] = at.oo[16 + i];
          snd[12 * 64] = c[1]; }
        lds_bar();
        const unsigned redo = lflag[0];
        { int lane = __builtin_amdgcn_mbcnt_hi(~0u, __builtin_amdgcn_mbcnt_lo(~0u, 0u)); asm volatile("" : "+v"(lane));
          const int r32 = lane & 31, hh = lane >> 5;
          float f[12];
          const LAS float* rcv = (const LAS float*)lds + (w ^ 4) * 13 * 64 + lane;
          const float co = rcv[12 * 64], cx = fmaxf(c[0], co) + 1.f;
          const float wa = __builtin_amdgcn_exp2f(c[0] - cx), wb = __builtin_amdgcn_exp2f(co - cx);
#pragma unroll
          for (int i = 0; i < 12; ++i) f[i] = at.oo[i] * wa + rcv[i * 64] * wb;
          const float inv = __builtin_amdgcn_rcpf(swap_add(hh ? f[8] : 0.f));
          const unsigned base = WS_HP + (16 * mq + 4 * qq + 2 * kh + (r32 >> 4)) * (272 * 256) + (r32 & 15) * 16;
          const u32x4 v = {pk2(f[0] * inv, f[1] * inv), pk2(f[2] * inv, f[3] * inv), pk2(f[4] * inv, f[5] * inv), pk2(f[6] * inv, f[7] * inv)};
          __builtin_amdgcn_raw_buffer_store_b128(v, wsr, base + (192 + 2 * g + hh) * 256, 0, SC1);
          const u32x2 v2 = {pk2(f[8] * inv, f[9] * inv), pk2(f[10] * inv, f[11] * inv)};
          __builtin_amdgcn_raw_buffer_store_b64(v2, wsr, hh ? OOB : base + (256 + (g >> 1)) * 256 + (g & 1) * 8, 0, SC1); }
        if (attempt || __builtin_amdgcn_readfirstlane(redo) == 0u) break;
        lds_bar();
        asm volatile("s_waitcnt vmcnt(0)" :: "{v[0:31]}"(pfa), "{v[32:63]}"(pfb), "{v[232:239]}"(pfc) : "memory");
        { int ln = __builtin_amdgcn_mbcnt_hi(~0u, __builtin_amdgcn_mbcnt_lo(~0u, 0u)); asm volatile("" : "+v"(ln));
          attn_prologue1(at, ws + kvoff, qp + kh * 2560, qp + (kh ^ 1) * 2560); attn_init_v(at, (ln & 31) == 20 ? 0x3f803f80u : 0u);
          attn_prologue2(at); attn_init_o(at); attn_init_s(at); }
        attn_first_scores<0>(at);
        c[0] = -INFINITY; c[1] = -INFINITY;
#pragma unroll 1
        for (int t = 0; t < 32; ++t) {
            const u32x4 km = __builtin_amdgcn_raw_buffer_load_b128(wsr, at.misc[1] - 10240, kvoff + t * 5120, SC1);
            const u32x2 kr = __builtin_amdgcn_raw_buffer_load_b64(wsr, at.misc[3] - 10240, kvoff + t * 5120, SC1);
#pragma unroll
            for (int i = 0; i < 2; ++i) {
                const u32x4 qm = {at.qm[4 * i], at.qm[4 * i + 1], at.qm[4 * i + 2], at.qm[4 * i + 3]};
                const u32x2 qr = {at.qr[2 * i], at.qr[2 * i + 1]};
                f32x16 s = MFMA32(__builtin_bit_cast(bf16x8, km), __builtin_bit_cast(bf16x8, qm), (f32x16){});
                s = MFMA8(kr, qr, s);
                c[i] = fmaxf(c[i], max16<0>(s));
            }
        }
    }
    LANE_IDS;
    auto loada = [&](int s, u32x4 (&a)[4]) {
#pragma unroll
        for (int i = 0; i < 4; ++i) a[i] = __builtin_amdgcn_raw_buffer_load_b128(wsr, lane * 16, aob + s * 1024 + i * (272 * 256), SC1);
    };
    auto loadb = [&](int s, u32x4 (&b)[5]) {
#pragma unroll
        for (int i = 0; i < 5; ++i) b[i] = __builtin_amdgcn_raw_buffer_load_b128(wsr, lane * 16, bob + s * 1024 + i * (68 * 1024), SC1);
    };
    STAMPP(1, 7); STAMPP(3, 11);

    f32x4 acc[4][5] = {};
    auto mma = [&](const u32x4 (&a)[4], const u32x4 (&b)[5]) {
#pragma unroll
        for (int i = 0; i < 4; ++i)
#pragma unroll
            for (int n = 0; n < 5; ++n) acc[i][n] = MFMA16(__builtin_bit_cast(bf16x8, b[n]), __builtin_bit_cast(bf16x8, a[i]), acc[i][n]);
    };
    u32x4 a0[4], b0[5], a1[4], b1[5], a2[4], b2[5], a3[4], b3[5];
    SBAR();
    asm volatile("s_waitcnt vmcnt(2)" : "+{v[0:31]}"(pfa), "+{v[32:63]}"(pfb), "+{v[232:239]}"(pfc) :: "memory");
#pragma unroll
    for (int i = 0; i < 4; ++i) {
        a0[i] = (u32x4){pfa[4 * i], pfa[4 * i + 1], pfa[4 * i + 2], pfa[4 * i + 3]}; a1[i] = (u32x4){pfa[16 + 4 * i], pfa[17 + 4 * i], pfa[18 + 4 * i], pfa[19 + 4 * i]};
        b0[i] = (u32x4){pfb[4 * i], pfb[4 * i + 1], pfb[4 * i + 2], pfb[4 * i + 3]}; b1[i] = (u32x4){pfb[16 + 4 * i], pfb[17 + 4 * i], pfb[18 + 4 * i], pfb[19 + 4 * i]};
    }
    b0[4] = (u32x4){pfc[0], pfc[1], pfc[2], pfc[3]}; b1[4] = (u32x4){pfc[4], pfc[5], pfc[6], pfc[7]};
    mma(a0, b0); SBAR();
    asm volatile("s_waitcnt vmcnt(0)" ::: "memory");
    arrive_publish(lflag + 2, lane, ws + WS_FO + (mq * 32 + g) * 128, tag);
    SBAR();
    loada(k2, a2); loadb(k2, b2); loada(k2 + 1, a3); loadb(k2 + 1, b3); loada(k4, a0); loadb(k4, b0); SBAR();
    mma(a1, b1); SBAR();
    loada(k4 + 1, a1); loadb(k4 + 1, b1); SBAR();
    mma(a2, b2); SBAR();
    loadb(48 + w, b2); SBAR();
    mma(a3, b3); SBAR();
    loadb(56 + w, b3); SBAR();
    mma(a0, b0); SBAR();
    loadb(64 + (w & 3), b0);
    u32x2 xv[3]; int xoff[3];
#pragma unroll
    for (int it = 0; it < 3; ++it) {
        const int tl = min(w + 8 * it, 19), i = tl / 5, n = tl - 5 * i;
        xoff[it] = (64 * rb + 16 * i + (lane & 15)) * 1280 + (80 * ct + 16 * n + 4 * (lane >> 4)) * 2;
        xv[it] = *(const GAS u32x2*)(x + xoff[it]);
    }
    SBAR();
    mma(a1, b1); SBAR();
    STAMPP(1, 8);
    flag_wait(ws, WS_FO + ((rb >> 2) * 32 + r32) * 128, (u32x2){~(unsigned)tag, 0u}, tag);
    STAMPP(1, 9);
    loada(48 + w, a2); loada(56 + w, a3); loada(64 + (w & 3), a0); SBAR();
    mma(a2, b2); mma(a3, b3);
    if (w < 4) mma(a0, b0);
    STAMPP(1, 10);
    lds_bar();
    LAS f32x4* red = (LAS f32x4*)lds;
#pragma unroll
    for (int i = 0; i < 4; ++i)
#pragma unroll
        for (int n = 0; n < 5; ++n) red[w * 1280 + (i * 5 + n) * 64 + lane] = acc[i][n];
    lds_bar();
#pragma unroll
    for (int it = 0; it < 3; ++it) {
        if (it < 2 || w < 4) {
            const int item = tid + 512 * it;
            f32x4 s = red[item];
#pragma unroll
            for (int k = 1; k < 8; ++k) s += red[k * 1280 + item];
            u32x2 v;
            v[0] = pk2(s[0] + __uint_as_float(xv[it][0] << 16), s[1] + __uint_as_float(xv[it][0] & 0xffff0000u));
            v[1] = pk2(s[2] + __uint_as_float(xv[it][1] << 16), s[3] + __uint_as_float(xv[it][1] & 0xffff0000u));
            *(GAS u32x2*)(out + xoff[it]) = v;
        }
    }
    STAMPP(1, 11);
    STAMP_OUT;
}

extern "C" void kernel_launch(void* const* d_in, const int* in_sizes, int n_in, void* d_out, int out_size, void* d_ws, size_t ws_size,
                              hipStream_t stream) {
    fused_block<<<256, 512, 0, stream>>>((const char*)d_in[0], (const char*)d_in[1], (const char*)d_in[2], (const char*)d_in[4],
                                         (const char*)d_in[5], (const char*)d_in[6], (char*)d_out, (char*)d_ws);
}
```

```cpp
#include <hip/hip_runtime.h>
#include <hip/hip_bf16.h>

#define STAMPS 0

typedef __bf16 bf16x8 __attribute__((ext_vector_type(8)));
typedef __bf16 bf16x2 __attribute__((ext_vector_type(2)));
typedef short s16x4 __attribute__((ext_vector_type(4)));
typedef float f32x32 __attribute__((ext_vector_type(32)));
typedef float f32x16 __attribute__((ext_vector_type(16)));
typedef float f32x4 __attribute__((ext_vector_type(4)));
typedef unsigned u32x32 __attribute__((ext_vector_type(32)));
typedef unsigned u32x16 __attribute__((ext_vector_type(16)));
typedef unsigned u32x8 __attribute__((ext_vector_type(8)));
typedef unsigned u32x4 __attribute__((ext_vector_type(4)));
typedef unsigned u32x2 __attribute__((ext_vector_type(2)));
typedef unsigned long long u64;
#define LAS __attribute__((address_space(3)))
#define GAS __attribute__((address_space(1)))
#define DEV __device__ __forceinline__

constexpr unsigned WS_EPOCH = 0;
constexpr unsigned WS_FA = 4096;
constexpr unsigned WS_FH = 8192;
constexpr unsigned WS_FO = 16384;
constexpr unsigned WS_GA = 65536;
constexpr unsigned WS_GB = WS_GA + 256 * 384 * 8;
constexpr unsigned WS_Q = WS_GB + 256 * 192 * 8;
constexpr unsigned KVS = 5120;
constexpr unsigned WS_KV = WS_Q + 16 * 64 * 2560;
constexpr unsigned WS_HP = WS_KV + 16 * 64 * KVS;
constexpr unsigned WS_WP = WS_HP + 128 * 272 * 256;
constexpr unsigned WS_END = WS_WP + 40 * 68 * 1024;
constexpr unsigned WS_STAMP = 32u << 20;
constexpr unsigned OOB = 0x40000000u;

constexpr int L_TR = 131072;
constexpr int L_PART = L_TR + 16384;
constexpr int L_FIN = L_PART + 6144;
constexpr int KVT = 2848;
constexpr int L_KV1 = 0;
constexpr int L_WO = 12 * KVT;
constexpr int L_MRG = L_WO + 102400;
constexpr int L_FLAG = L_MRG + 26624;
constexpr int L_TOTAL = 163840;

constexpr float QSCALE = 0.22360679774997896f * 1.4426950408889634f;
constexpr int SC1 = 16;
#define NT0 32
constexpr unsigned SPIN_CAP = 1u << 22;

DEV int lane_id() {
    int l; asm volatile("v_mbcnt_lo_u32_b32 %0, -1, 0\n\tv_mbcnt_hi_u32_b32 %0, -1, %0" : "=v"(l)); return l;
}
DEV unsigned pk2(float lo, float hi) { bf16x2 v = {(__bf16)lo, (__bf16)hi}; return __builtin_bit_cast(unsigned, v); }
DEV float bfr(float x) { return (float)(__bf16)x; }
DEV u32x2 split3(float v) {
    const float a = bfr(v), b = bfr(v - a), c = v - a - b;
    return (u32x2){pk2(a, b), pk2(c, 0.f)};
}
DEV float swap_add(float v) {
    auto r = __builtin_amdgcn_permlane32_swap(__float_as_uint(v), __float_as_uint(v), false, false);
    return __uint_as_float(r[0]) + __uint_as_float(r[1]);
}
DEV float swap_max(float v) {
    auto r = __builtin_amdgcn_permlane32_swap(__float_as_uint(v), __float_as_uint(v), false, false);
    return fmaxf(__uint_as_float(r[0]), __uint_as_float(r[1]));
}
DEV void glds16(unsigned voff, const void* sbase, unsigned ldsdst) {
    asm volatile("s_mov_b32 m0, %2\n\ts_nop 0\n\tglobal_load_lds_dwordx4 %0, %1" ::"v"(voff), "s"(sbase), "s"(ldsdst) : "memory");
}
DEV void glds16_dev(unsigned voff, const void* sbase, unsigned ldsdst) {
    asm volatile("s_mov_b32 m0, %2\n\ts_nop 0\n\tglobal_load_lds_dwordx4 %0, %1 sc1" ::"v"(voff), "s"(sbase), "s"(ldsdst) : "memory");
}
template <int N> DEV void wait_bar() { asm volatile("s_waitcnt vmcnt(%0) lgkmcnt(0)\n\ts_barrier" ::"i"(N) : "memory"); }
DEV void lds_bar() { asm volatile("s_waitcnt lgkmcnt(0)\n\ts_barrier" ::: "memory"); }
DEV bf16x8 lds128(const LAS char* p) { return *(const LAS bf16x8*)p; }
DEV bf16x8 ldstr(const LAS char* p0, const LAS char* p1) {
    s16x4 a = __builtin_amdgcn_ds_read_tr16_b64_v4i16((LAS s16x4*)p0), b = __builtin_amdgcn_ds_read_tr16_b64_v4i16((LAS s16x4*)p1);
    return __builtin_bit_cast(bf16x8, __builtin_shufflevector(a, b, 0, 1, 2, 3, 4, 5, 6, 7));
}
DEV float dot_ones(bf16x8 v, float acc, unsigned ones = 0x3f803f80u) {
    const bf16x2 one = __builtin_bit_cast(bf16x2, ones);
#pragma unroll
    for (int i = 0; i < 4; ++i) acc = __builtin_amdgcn_fdot2_f32_bf16((bf16x2){v[2 * i], v[2 * i + 1]}, one, acc, false);
    return acc;
}
DEV float dot_self(bf16x8 v, float acc) {
#pragma unroll
    for (int i = 0; i < 4; ++i) { bf16x2 p = {v[2 * i], v[2 * i + 1]}; acc = __builtin_amdgcn_fdot2_f32_bf16(p, p, acc, false); }
    return acc;
}
#define MFMA32(a, b, c) __builtin_amdgcn_mfma_f32_32x32x16_bf16(a, b, c, 0, 0, 0)
#define MFMA8(a, b, c) __builtin_amdgcn_mfma_f32_32x32x8bf16_1k(__builtin_bit_cast(s16x4, a), __builtin_bit_cast(s16x4, b), c, 0, 0, 0)
#define MFMA16(a, b, c) __builtin_amdgcn_mfma_f32_16x16x32_bf16(a, b, c, 0, 0, 0)
#define SBAR() __builtin_amdgcn_sched_barrier(0)

#define STAMP(i) do {} while (0)
#define STAMPP(p, i) do {} while (0)
#define STAMP_DECL
#define STAMP_OUT do {} while (0)

DEV u32x2 ld2_async(const char* base, unsigned off) { u32x2 v; asm volatile("global_load_dwordx2 %0, %1, %2 sc1" : "=v"(v) : "v"(off), "s"(base) : "memory"); return v; }
DEV u32x4 ld4_async(const char* base, unsigned off) { u32x4 v; asm volatile("global_load_dwordx4 %0, %1, %2 sc1" : "=v"(v) : "v"(off), "s"(base) : "memory"); return v; }
DEV void flag_store(char* p, u64 tag) { __hip_atomic_store((GAS u64*)p, tag, __ATOMIC_RELAXED, __HIP_MEMORY_SCOPE_AGENT); }
DEV float gran_sum(const char* ws, unsigned off, unsigned stride, u32x2 (&g)[4], unsigned tag) {
    if (__builtin_expect(__any(((g[0][1] ^ tag) | (g[1][1] ^ tag) | (g[2][1] ^ tag) | (g[3][1] ^ tag)) != 0u), 0)) {
#pragma unroll 1
        for (unsigned spin = 0; spin < SPIN_CAP; ++spin) {
            asm volatile("s_sleep 1\n\tglobal_load_dwordx2 %0, %4, %8 sc1\n\tglobal_load_dwordx2 %1, %5, %8 sc1\n\t"
                         "global_load_dwordx2 %2, %6, %8 sc1\n\tglobal_load_dwordx2 %3, %7, %8 sc1\n\ts_waitcnt vmcnt(0)"
                         : "=&v"(g[0]), "=&v"(g[1]), "=&v"(g[2]), "=&v"(g[3])
                         : "v"(off), "v"(off + stride), "v"(off + 2 * stride), "v"(off + 3 * stride), "s"(ws) : "memory");
            if (__all(g[0][1] == tag && g[1][1] == tag && g[2][1] == tag && g[3][1] == tag)) break;
        }
    }
    return (__uint_as_float(g[0][0]) + __uint_as_float(g[1][0])) + (__uint_as_float(g[2][0]) + __uint_as_float(g[3][0]));
}
DEV void flag_poll(const char* ws, unsigned off, u64 tag) {
#pragma unroll 1
    for (unsigned spin = 0; spin < SPIN_CAP; ++spin) {
        u64 f;
        asm volatile("global_load_dwordx2 %0, %1, %2 sc1\n\ts_waitcnt vmcnt(0)" : "=&v"(f) : "v"(off), "s"(ws) : "memory");
        if (__all(f == tag)) break;
        __builtin_amdgcn_s_sleep(1);
    }
}
DEV void flag_wait(const char* ws, unsigned off, u32x2 f, u64 tag) {
    if (__builtin_expect(__any(__builtin_bit_cast(u64, f) != tag), 0)) flag_poll(ws, off, tag);
}
DEV void flag_wait4(const char* ws, unsigned off, u32x4 f, u32x4 g, u64 tag) {
    const unsigned lo = (unsigned)tag, hi = (unsigned)(tag >> 32);
#pragma unroll 1
    for (unsigned spin = 0; spin < SPIN_CAP; ++spin) {
        if (__all(((f[0] ^ lo) | (f[1] ^ hi) | (f[2] ^ lo) | (f[3] ^ hi) | (g[0] ^ lo) | (g[1] ^ hi) | (g[2] ^ lo) | (g[3] ^ hi)) == 0u)) break;
        asm volatile("s_sleep 1\n\tglobal_load_dwordx4 %0, %2, %3 sc1\n\tglobal_load_dwordx4 %1, %2, %3 offset:1024 sc1\n\ts_waitcnt vmcnt(0)"
                     : "=&v"(f), "=&v"(g) : "v"(off), "s"(ws) : "memory");
    }
}
DEV void arrive_publish(LAS unsigned* cnt, unsigned last, int lane, char* flag, u64 tag) {
    if (lane == 0) {
        const unsigned old = __hip_atomic_fetch_add(cnt, 1u, __ATOMIC_RELAXED, __HIP_MEMORY_SCOPE_WORKGROUP);
        if (old == last) flag_store(flag, tag);
    }
}

#define EXP4(a, b, c, d) "v_exp_f32 v" #a ", v" #a "\n\tv_exp_f32 v" #b ", v" #b "\n\tv_exp_f32 v" #c ", v" #c "\n\tv_exp_f32 v" #d ", v" #d "\n\t"
#define E_S0_0 EXP4(64, 65, 66, 67)
#define E_S0_1 EXP4(68, 69, 70, 71)
#define E_S0_2 EXP4(72, 73, 74, 75)
#define E_S0_3 EXP4(76, 77, 78, 79)
#define E_S1_0 EXP4(80, 81, 82, 83)
#define E_S1_1 EXP4(84, 85, 86, 87)
#define E_S1_2 EXP4(88, 89, 90, 91)
#define E_S1_3 EXP4(92, 93, 94, 95)
#define E_S2_0 EXP4(96, 97, 98, 99)
#define E_S2_1 EXP4(100, 101, 102, 103)
#define E_S2_2 EXP4(104, 105, 106, 107)
#define E_S2_3 EXP4(108, 109, 110, 111)
#define E_S3_0 EXP4(112, 113, 114, 115)
#define E_S3_1 EXP4(116, 117, 118, 119)
#define E_S3_2 EXP4(120, 121, 122, 123)
#define E_S3_3 EXP4(124, 125, 126, 127)
#define R_S0 "v[64:79]"
#define R_S1 "v[80:95]"
#define R_S2 "v[96:111]"
#define R_S3 "v[112:127]"
#define R_S0L "v[64:71]"
#define R_S0H "v[72:79]"
#define R_S1L "v[80:87]"
#define R_S1H "v[88:95]"
#define R_S2L "v[96:103]"
#define R_S2H "v[104:111]"
#define R_S3L "v[112:119]"
#define R_S3H "v[120:127]"
#define R_O0 "v[128:143]"
#define R_O1 "v[144:159]"
#define R_V0A "v[160:163]"
#define R_V0B "v[164:167]"
#define R_V1A "v[168:171]"
#define R_V1B "v[172:175]"
#define R_K0M "v[176:179]"
#define R_K1M "v[180:183]"
#define R_K0R "v[184:185]"
#define R_K1R "v[186:187]"
#define R_Q0M "v[192:195]"
#define R_Q1M "v[196:199]"
#define R_Q0R "v[200:201]"
#define R_Q1R "v[202:203]"
#define QK1(s, k, q) "v_mfma_f32_32x32x16_bf16 " R_S##s ", " R_K##k##M ", " R_Q##q##M ", 0\n\t"
#define QK2(s, k, q) "v_mfma_f32_32x32x8_bf16 " R_S##s ", " R_K##k##R ", " R_Q##q##R ", " R_S##s "\n\t"
#define PV1(o, v, s) "v_smfmac_f32_32x32x32_bf16 " R_O##o ", " R_V##v##A ", " R_S##s##L ", v204\n\t"
#define PV2(o, v, s) "v_smfmac_f32_32x32x32_bf16 " R_O##o ", " R_V##v##B ", " R_S##s##H ", v204\n\t"
#define PERIOD(e, sn, k, q, sp, v, o) \
    QK1(sn, k, q) E_S##e##_0 PV1(o, v, sp) E_S##e##_1 QK2(sn, k, q) E_S##e##_2 PV2(o, v, sp) E_S##e##_3
#define PERIOD_NOQK(e, sp, v, o) E_S##e##_0 PV1(o, v, sp) E_S##e##_1 E_S##e##_2 PV2(o, v, sp) E_S##e##_3
#define PERIOD_NOPV(e, sn, k, q) QK1(sn, k, q) E_S##e##_0 E_S##e##_1 QK2(sn, k, q) E_S##e##_2 E_S##e##_3
#define RDK(k, o) "ds_read_b128 " R_K##k##M ", v188 offset:" #o "\n\tds_read_b64 " R_K##k##R ", v189 offset:" #o "\n\t"
#define RDV(v, o) "ds_read_b128 " R_V##v##A ", v190 offset:" #o "\n\tds_read_b128 " R_V##v##B ", v191 offset:" #o "\n\t"
#define LGK0 "s_waitcnt lgkmcnt(0)\n\t"
#define FETCH "s_add_u32 m0, s62, %[lb]\n\ts_addk_i32 s62, 11392\n\tbuffer_load_dwordx4 v205, %[srd], s60 offen sc1 lds\n\t" \
              "buffer_load_dwordx4 v206, %[srd], s60 offen offset:1024 sc1 lds\n\ts_mov_b32 exec_hi, 0xffff\n\t" \
              "buffer_load_dwordx4 v207, %[srd], s60 offen offset:2048 sc1 lds\n\ts_mov_b32 exec_hi, -1\n\t" \
              "s_addk_i32 s60, 20480\n\ts_cmp_eq_u32 s62, 34176\n\ts_cselect_b32 s62, 0, s62\n\t"
#define NOFETCH "s_addk_i32 s62, 11392\n\ts_cmp_eq_u32 s62, 34176\n\ts_cselect_b32 s62, 0, s62\n\t"
#define ARRIVE(n) "s_waitcnt vmcnt(" n ")\n\ts_mov_b64 exec, 1\n\tds_add_u32 v208, v209\n\ts_mov_b64 exec, -1\n\ts_addk_i32 s61, 4\n\t"
#define PEEK "ds_read_b32 v210, v208\n\t"
#define SYNC(lab) lab ":\n\tv_readfirstlane_b32 s63, v210\n\ts_cmp_ge_u32 s63, s61\n\ts_cbranch_scc1 " lab "1f\n\t" \
                  "s_sleep 1\n\t" PEEK LGK0 "s_branch " lab "b\n\t" lab "1:\n\t" \
                  "s_cmp_eq_u32 s62, 11392\n\ts_cselect_b32 s63, %[dn], %[up]\n\tv_add_u32 v188, s63, v188\n\tv_add_u32 v189, s63, v189\n\t"
#define VNEXT "v_add_u32 v190, s63, v190\n\tv_add_u32 v191, s63, v191\n\t"
#define PE0 PERIOD(0, 2, 1, 0, 3, 1, 1)
#define PE1 PERIOD(1, 3, 1, 1, 0, 0, 0)
#define PE2 PERIOD(2, 0, 0, 0, 1, 0, 1)
#define PE3 PERIOD(3, 1, 0, 1, 2, 1, 0)
#define ITER_X(P0, lab, na, F, e0, e1, e2, e3) \
        LGK0 e0 P0 RDK(0, 5696) RDV(1, 2848) PE1 \
        LGK0 ARRIVE(na) e1 PE2 RDK(1, 8544) RDV(0, 5696) PEEK PE3 \
        LGK0 e2 PE0 SYNC(lab) RDK(0, 0) RDV(1, 8544) F PE1 \
        LGK0 VNEXT e3 PE2 RDK(1, 2848) RDV(0, 0) PE3
#define ITER_LAST(e0, e1, e2, e3) \
        LGK0 e0 PE0 RDK(0, 5696) RDV(1, 2848) PE1 \
        LGK0 e1 PE2 RDK(1, 8544) RDV(0, 5696) PE3 \
        LGK0 e2 PE0 RDV(1, 8544) PE1 \
        LGK0 e3 PERIOD_NOQK(2, 1, 0, 1) PERIOD_NOQK(3, 2, 1, 0) "s_nop 1\n\t" PV1(1, 1, 3) PV2(1, 1, 3) "s_nop 15\n\t"

struct Attn { f32x32 sa, sb, oo; u32x32 kv; u32x8 qm, con; u32x4 qr, srd, ss; unsigned lb; };
#define ATTN_DMA(a) "{v[204:211]}"(a.con), [srd] "s"(a.srd), [lb] "s"(a.lb)
DEV void attn_load_q(Attn& a, unsigned q, unsigned vqm, unsigned vqr) {
    asm volatile("buffer_load_dwordx4 " R_Q0M ", %[m], %[srd], %[q] offen sc1\n\tbuffer_load_dwordx4 " R_Q1M ", %[m], %[srd], %[q] offen offset:2560 sc1\n\t"
                 "buffer_load_dwordx2 " R_Q0R ", %[r], %[srd], %[q] offen sc1\n\tbuffer_load_dwordx2 " R_Q1R ", %[r], %[srd], %[q] offen offset:2560 sc1"
                 : "=&{v[192:199]}"(a.qm), "=&{v[200:203]}"(a.qr) : [srd] "s"(a.srd), [q] "s"(q), [m] "v"(vqm), [r] "v"(vqr) : "memory");
}
DEV void attn_fetch(Attn& a) { asm volatile(FETCH : "+{s[60:63]}"(a.ss) : ATTN_DMA(a) : "memory", "scc"); }
#define ZERO8(r) "v_mov_b32 v[" #r "], 0\n\tv_mov_b32 v[" #r "+1], 0\n\tv_mov_b32 v[" #r "+2], 0\n\tv_mov_b32 v[" #r "+3], 0\n\t" \
                 "v_mov_b32 v[" #r "+4], 0\n\tv_mov_b32 v[" #r "+5], 0\n\tv_mov_b32 v[" #r "+6], 0\n\tv_mov_b32 v[" #r "+7], 0"
template <int P> DEV void attn_init_o(Attn& a) {
    if constexpr (P == 0) asm volatile(ZERO8(128) : "={v[128:159]}"(a.oo));
    if constexpr (P == 1) asm volatile(ZERO8(136) : "+{v[128:159]}"(a.oo));
    if constexpr (P == 2) asm volatile(ZERO8(144) : "+{v[128:159]}"(a.oo));
    if constexpr (P == 3) asm volatile(ZERO8(152) : "+{v[128:159]}"(a.oo));
}
#define SCORES0 QK1(0, 0, 0) QK1(1, 0, 1) QK2(0, 0, 0) QK2(1, 0, 1) "s_nop 11\n\t"
template <int N> DEV void attn_begin(Attn& a) {
    asm volatile("s_waitcnt vmcnt(%[n]) lgkmcnt(0)\n\ts_barrier\n\t" RDK(0, 0) RDK(1, 2848) RDV(0, 0) "s_waitcnt lgkmcnt(4)\n\t" SCORES0 LGK0
                 : "=&{v[64:95]}"(a.sa), "+{v[160:191]}"(a.kv), "+{v[192:199]}"(a.qm), "+{v[200:203]}"(a.qr) : [n] "i"(N) : "memory");
}
DEV void attn_scores0(Attn& a) {
    asm volatile("s_nop 1\n\t" SCORES0 : "=&{v[64:95]}"(a.sa) : "{v[160:191]}"(a.kv), "{v[192:199]}"(a.qm), "{v[200:203]}"(a.qr));
}
#define ATTN_STATE2(a) "+{v[64:95]}"(a.sa), "+{v[128:159]}"(a.oo), "+{v[160:191]}"(a.kv), "+{s[60:63]}"(a.ss), "+{v[204:211]}"(a.con)
#define ATTN_STATE(a) ATTN_STATE2(a), "+{v[96:127]}"(a.sb)
#define ATTN_IN(a) "{v[192:199]}"(a.qm), "{v[200:203]}"(a.qr), [srd] "s"(a.srd), [lb] "s"(a.lb), [dn] "s"(-8 * KVT), [up] "s"(4 * KVT)
template <int X> DEV void attn_iter_first(Attn& a) {
    asm volatile(ITER_X(PERIOD_NOPV(0, 2, 1, 0), "1", "%[n]", FETCH, , , , )
                 : ATTN_STATE2(a), "=&{v[96:127]}"(a.sb) : ATTN_IN(a), [n] "i"(X) : "memory", "scc");
}
template <int X> DEV void attn_iter(Attn& a) {
    asm volatile(ITER_X(PE0, "1", "%[n]", FETCH, , , , ) "s_waitcnt vmcnt(3)" : ATTN_STATE(a) : ATTN_IN(a), [n] "i"(X) : "memory", "scc");
}
DEV void attn_loop(Attn& a, unsigned n) {
    asm volatile("s_cmp_eq_u32 %[n], 0\n\ts_cbranch_scc1 3f\n\t2:\n\t" ITER_X(PE0, "1", "0", FETCH, , , , )
                 "s_sub_u32 %[n], %[n], 1\n\ts_cmp_lg_u32 %[n], 0\n\ts_cbranch_scc1 2b\n\t3:"
                 : ATTN_STATE(a), [n] "+s"(n) : ATTN_IN(a) : "memory", "scc");
}
#define PF1(r, p, o) "buffer_load_dwordx4 " r ", %[v], %[srd], " p " offen" o " sc1\n\t"
#define PF2(r0, r1, p) PF1(r0, p, "") PF1(r1, p, " offset:1024")
#define PFA "s_add_u32 %[pa], %[pa], 69632\n\t"
#define PFB "s_add_u32 %[pb], %[pb], 69632\n\t"
#define ONLY0(x) "s_cmp_lg_u32 %[kh], 0\n\ts_cbranch_scc1 9f\n\t" x "9:\n\t"
DEV void attn_last(Attn& a, int kh, u32x32& pfa, u32x32& pfb, u32x8& pfc, unsigned pa, unsigned pb, unsigned voff) {
    asm volatile(
        ITER_X(PE0, "1", "0", NOFETCH, , ONLY0(PF2("v[0:3]", "v[16:19]", "%[pa]") PFA PF2("v[4:7]", "v[20:23]", "%[pa]") PFA),
               ONLY0(PF2("v[8:11]", "v[24:27]", "%[pa]") PFA PF2("v[12:15]", "v[28:31]", "%[pa]")), ONLY0(PF2("v[32:35]", "v[48:51]", "%[pb]") PFB))
        ITER_LAST(ONLY0(PF2("v[36:39]", "v[52:55]", "%[pb]") PFB PF2("v[40:43]", "v[56:59]", "%[pb]") PFB),
                  ONLY0(PF2("v[44:47]", "v[60:63]", "%[pb]") PFB PF2("v[212:215]", "v[216:219]", "%[pb]")), , )
        : ATTN_STATE(a), "=&{v[0:31]}"(pfa), "=&{v[32:63]}"(pfb), "=&{v[212:219]}"(pfc), [pa] "+s"(pa), [pb] "+s"(pb)
        : ATTN_IN(a), [v] "v"(voff), [kh] "s"(kh)
        : "memory", "scc");
}
template <int O, class V> DEV float max16(const V& s) {
    float a = fmaxf(fmaxf(s[O], s[O + 1]), s[O + 2]), b = fmaxf(fmaxf(s[O + 3], s[O + 4]), s[O + 5]);
    a = fmaxf(fmaxf(a, s[O + 6]), s[O + 7]); b = fmaxf(fmaxf(b, s[O + 8]), s[O + 9]);
    a = fmaxf(fmaxf(a, s[O + 10]), s[O + 11]); b = fmaxf(fmaxf(b, s[O + 12]), s[O + 13]);
    a = fmaxf(fmaxf(a, s[O + 14]), s[O + 15]);
    return fmaxf(a, b);
}
DEV void attn_set_reference(Attn& a, float (&c)[2], int hh) {
#pragma unroll
    for (int i = 0; i < 2; ++i) {
        c[i] = swap_max(c[i]);
        const u32x2 t = split3(-c[i]);
        a.qr[2 * i] = hh ? t[0] : a.qr[2 * i]; a.qr[2 * i + 1] = hh ? t[1] : a.qr[2 * i + 1];
    }
}

typedef __bf16 bf16x16 __attribute__((ext_vector_type(16)));
typedef float f32x8 __attribute__((ext_vector_type(8)));
DEV void attn_check_redo(Attn& a, float (&c)[2], __amdgpu_buffer_rsrc_t wsr, unsigned kvoff, int odd, int ntiles) {
    float chk0 = (a.oo[8] * 0x1p30f) * 0.f, chk1 = (a.oo[24] * 0x1p30f) * 0.f;
#pragma unroll
    for (int i = 0; i < 12; ++i) { chk0 = __builtin_fmaf(a.oo[i], 0.f, chk0); chk1 = __builtin_fmaf(a.oo[16 + i], 0.f, chk1); }
    if (__builtin_expect(__any(!(chk0 + chk1 == 0.f)), 0)) {
        asm volatile("s_waitcnt vmcnt(0)" ::: "memory");
        const int lane = lane_id(), r32 = lane & 31, hh = lane >> 5;
        const unsigned vk = odd * 1536 + lane * 16, vr = hh ? OOB : 1024 + r32 * 16 + odd * 8, vv = r32 < 20 ? 2560 + odd * 1280 + hh * 320 + r32 * 16 : OOB;
        const unsigned one = r32 == 20 ? 0x3f803f80u : 0u;
        const u32x4 qm[2] = {{a.qm[0], a.qm[1], a.qm[2], a.qm[3]}, {a.qm[4], a.qm[5], a.qm[6], a.qm[7]}};
        const u32x2 qr[2] = {{hh ? 0u : a.qr[0], hh ? 0u : a.qr[1]}, {hh ? 0u : a.qr[2], hh ? 0u : a.qr[3]}};
        f32x16 o[2] = {};
        c[0] = -INFINITY; c[1] = -INFINITY;
#pragma unroll 1
        for (int pass = 0; pass < 2; ++pass) {
#pragma unroll 1
            for (int t = 0; t < ntiles; ++t) {
                const u32x4 km = __builtin_amdgcn_raw_buffer_load_b128(wsr, vk, kvoff + t * KVS, SC1);
                const u32x2 kr = __builtin_amdgcn_raw_buffer_load_b64(wsr, vr, kvoff + t * KVS, SC1);
                const u32x4 va = __builtin_amdgcn_raw_buffer_load_b128(wsr, vv, kvoff + t * KVS, SC1) | one;
                const u32x4 vb = __builtin_amdgcn_raw_buffer_load_b128(wsr, vv + 640, kvoff + t * KVS, SC1) | one;
#pragma unroll
                for (int i = 0; i < 2; ++i) {
                    f32x16 s = MFMA32(__builtin_bit_cast(bf16x8, km), __builtin_bit_cast(bf16x8, qm[i]), (f32x16){});
                    s = MFMA8(kr, qr[i], s);
                    if (pass == 0) c[i] = fmaxf(c[i], max16<0>(s));
                    else {
                        f32x8 pl, ph;
#pragma unroll
                        for (int e = 0; e < 8; ++e) { pl[e] = __builtin_amdgcn_exp2f(s[e] - c[i]); ph[e] = __builtin_amdgcn_exp2f(s[8 + e] - c[i]); }
                        o[i] = __builtin_amdgcn_smfmac_f32_32x32x32_bf16(__builtin_bit_cast(bf16x8, va), __builtin_bit_cast(bf16x16, pl), o[i], 0xdddd, 0, 0);
                        o[i] = __builtin_amdgcn_smfmac_f32_32x32x32_bf16(__builtin_bit_cast(bf16x8, vb), __builtin_bit_cast(bf16x16, ph), o[i], 0xdddd, 0, 0);
                    }
                }
            }
            c[0] = swap_max(c[0]); c[1] = swap_max(c[1]);
        }
#pragma unroll
        for (int e = 0; e < 16; ++e) { a.oo[e] = o[0][e]; a.oo[16 + e] = o[1][e]; }
    }
}

struct Ring {
    const char *x, *wq, *wgu;
    unsigned xo0, xo1, wo0, wo1, go0, go1, go2;
    unsigned lds0; int w, s0;
};
template <int C, int P> DEV void ring_piece(const Ring& r) {
    if constexpr (C < 20) {
        int cc = r.s0 + C % 10; cc = cc >= 10 ? cc - 10 : cc;
        if constexpr (C < 10) {
            const unsigned d = r.lds0 + (C & 3) * 32768 + r.w * 2048;
            if constexpr (P == 0) glds16(r.xo0, r.x + cc * 128, d);
            if constexpr (P == 1) glds16(r.xo1, r.x + cc * 128, d + 1024);
            if constexpr (P == 2) glds16(r.wo0, r.wq + cc * (64 * 3840), d + 16384);
            if constexpr (P == 3) glds16(r.wo1, r.wq + cc * (64 * 3840), d + 16384 + 1024);
        } else {
            const unsigned d = r.lds0 + (C & 3) * 32768 + r.w * 3072 + (r.w >> 2) * 32;
            const char* b = r.wgu + cc * (64 * 3072);
            if constexpr (P == 0) glds16(r.go0, b, d);
            if constexpr (P == 1) glds16(r.go1, b, d + 1024);
            if constexpr (P == 2) glds16(r.go2, b, d + 2048);
        }
    }
}
template <int C> DEV void ring_issue(const Ring& r) { ring_piece<C, 0>(r); ring_piece<C, 1>(r); ring_piece<C, 2>(r); ring_piece<C, 3>(r); }
constexpr int ring_n(int c) { return c < 10 ? 4 : (c < 20 ? 3 : 0); }
constexpr int vm_pre(int c) { return c == 6 ? 4 : (c == 15 ? 5 : 0); }
constexpr int vm_post(int c) { return ((c == 2 || c == 11) ? 1 : 0) + (c == 17 ? 7 : 0); }
constexpr int vm_wait(int c) {
    if (c == 9) return 63;
    if (c == 10) return 4 + 6;
    if (c == 11) return 6;
    return vm_post(c - 3) + vm_pre(c - 2) + ring_n(c + 2) + vm_post(c - 2) + vm_pre(c - 1) + ring_n(c + 3) + vm_post(c - 1);
}

__global__ __launch_bounds__(512, 2) void fused_block(const char* x, const char* wqkv, const char* wo, const char* wg, const char* wu, const char* wd,
                                                      char* out, char* ws) {
    __shared__ __attribute__((aligned(16))) char smem[L_TOTAL];
    LAS char* lds = (LAS char*)smem;
    const int w = __builtin_amdgcn_readfirstlane(threadIdx.x >> 6);
    const int bid = blockIdx.x, xcd = bid & 7, rr = bid >> 3;
#define LANE_IDS const int lane = lane_id(); \
    const int tid = 64 * w + lane, r32 = lane & 31, hh = lane >> 5; (void)tid; (void)r32; (void)hh
    asm volatile("" : "+s"(x), "+s"(wqkv), "+s"(wo), "+s"(wg), "+s"(wu), "+s"(wd), "+s"(out), "+s"(ws));
    const __amdgpu_buffer_rsrc_t wsr = __builtin_amdgcn_make_buffer_rsrc(ws, 0, WS_END, 0x00020000);
    LAS unsigned* lflag = (LAS unsigned*)(lds + L_FLAG);
    u64 tag;
    STAMP_DECL;
    STAMP(0);
    const int mi = rr & 7, ji = rr >> 3, m = 8 * (xcd & 1) + mi, j = 4 * (xcd >> 1) + ji;
    const int wm = w & 3, wn = w >> 2;
    const int g = 4 * xcd + (rr >> 3), mq = rr & 7, qq = w & 3, kh = w >> 2, odd = g & 1;
    const unsigned qoff = WS_Q + ((g >> 1) * 64 + 8 * mq + 2 * qq) * 2560;
    const unsigned kvoff = WS_KV + ((g >> 1) * 64 + NT0 * kh) * KVS;
    Attn at;
    at.srd = (u32x4){(unsigned)(size_t)ws, (unsigned)((size_t)ws >> 32) & 0xffffu, WS_END, 0x00020000u};
    float c[2];
    {
        LANE_IDS;
        Ring rg;
        rg.x = x + m * 128 * 1280; rg.wq = wqkv + (j < 15 ? j : 14) * 256; rg.wgu = (w < 4 ? wg : wu) + j * 192;
        rg.lds0 = (unsigned)(size_t)lds; rg.w = w;
        rg.s0 = (((5 * ji + 1) >> 1) + mi + (mi >> 2)) % 10;
        rg.xo0 = (16 * w + (lane >> 3)) * 1280 + (((lane & 7) ^ (lane >> 4)) << 4);
        rg.xo1 = (16 * w + 8 + (lane >> 3)) * 1280 + (((lane & 7) ^ (4 + (lane >> 4))) << 4);
        rg.wo0 = (8 * w + (lane >> 4)) * 3840 + (((lane & 15) ^ ((lane >> 4) << 2)) << 4);
        rg.wo1 = rg.wo0 + 4 * 3840;
        ring_issue<0>(rg); __builtin_amdgcn_s_barrier();
        ring_issue<1>(rg);
        asm volatile("s_load_dwordx2 %0, %1, 0x0\n\ts_waitcnt lgkmcnt(0)" : "=s"(tag) : "s"(ws) : "memory");
        tag += 1;
        const unsigned tag32 = (unsigned)tag;
        { const int p0 = (w & 3) * 192 + lane, p1 = p0 + 64, p2 = p0 + 128;
          rg.go0 = (p0 / 12) * 3072 + (p0 % 12) * 16; rg.go1 = (p1 / 12) * 3072 + (p1 % 12) * 16; rg.go2 = (p2 / 12) * 3072 + (p2 % 12) * 16; }
        if (tid < 8) lflag[tid] = 0u;

        const int g4 = lane >> 4, q4 = (lane >> 2) & 3, p4 = lane & 3, rot = wm ^ (2 * wn);
        const int xrow = 32 * wm + r32;
        const unsigned xa = (xrow * 128 + ((hh ^ ((xrow >> 1) & 7)) << 4)) ^ (rot << 5);
        const unsigned wab = 16384 + (rot << 12) + (8 * (g4 >> 1) + q4) * 256 + (g4 & 1) * 32 + (p4 & 1) * 16 + (p4 >> 1) * 8;
        const unsigned wa0 = wab + (((2 * wn) ^ q4) << 6), wa1 = wab + (((2 * wn + 1) ^ q4) << 6);
        LAS float* part = (LAS float*)(lds + L_PART); LAS u32x2* fin = (LAS u32x2*)(lds + L_FIN);
        bf16x8 xf[40], fw[2][3];
        f32x16 acc[2] = {};
        float rs = 0.f, rq = 0.f, cs0 = 0.f, cs1 = 0.f, pv[4];
        u32x2 gr[4], fa;
        const unsigned col2 = (mi & 3) == 3 ? 0x3f803f80u : 0u;
        int pw_row = (2 * wn + hh) * 384 + xrow, pw_col = wm * 384 + 256 + 64 * wn + lane, pr_i = w < 6 ? tid : lane;
        unsigned pub_o = tid * 8 + (w < 6 ? WS_GA + bid * 3072 : OOB);
        asm volatile("" : "+v"(pw_row), "+v"(pw_col), "+v"(pr_i), "+v"(pub_o), "+v"(rg.go0), "+v"(rg.go1), "+v"(rg.go2));
        const unsigned gab = w < 4 ? WS_GA + (xcd + 8 * mi) * 3072 + w * 512 : WS_GA + (xcd + 64 * ji + 32 * ((w >> 1) & 1)) * 3072 + 2048 + (w & 1) * 512;
        const unsigned gao = gab + lane * 8, gas = w < 4 ? 64 * 3072 : 8 * 3072;
        unsigned wadr[2][4], xadr[4];
#pragma unroll
        for (int k = 0; k < 4; ++k) { wadr[0][k] = wa0 ^ (k << 12); wadr[1][k] = wa1 ^ (k << 12); xadr[k] = xa ^ (k << 5); }
#define FLIP(c, a) do { if constexpr ((c) > 0 && ((c) & 1) == 0) a ^= 0x10000u; if constexpr (((c) & 1) == 0) asm volatile("" : "+v"(a)); } while (0)
#define RW1(f, b, c, k) FLIP(c, wadr[b][k]); fw[f][b] = ldstr(lds + ((c) & 1) * 32768 + wadr[b][k], lds + ((c) & 1) * 32768 + wadr[b][k] + 1024)
#define RX1(c, k) FLIP(c, xadr[k]); xf[4 * (c) + (k)] = lds128(lds + ((c) & 1) * 32768 + xadr[k])
#define M1(f, b, c, k) acc[b] = MFMA32(fw[f][b], xf[4 * (c) + (k)], acc[b]); SBAR()
        STAMPP(2, 1);
        wait_bar<4>();
        STAMPP(2, 2);
        RW1(0, 0, 0, 0); RX1(0, 0); RW1(0, 1, 0, 0); RW1(1, 0, 0, 1); RX1(0, 1); RW1(1, 1, 0, 1); SBAR();
        ring_issue<2>(rg); ring_piece<3, 0>(rg); SBAR();
#define A1C(c, H3, H4, H5, H6) do { constexpr int n_ = c < 9 ? c + 1 : 0; \
        M1(0, 0, c, 0); RW1(0, 0, c, 2); RX1(c, 2); if constexpr (c < 9) ring_piece<c + 3, 1>(rg); SBAR(); \
        M1(0, 1, c, 0); RW1(0, 1, c, 2); if constexpr (c < 9) ring_piece<c + 3, 2>(rg); SBAR(); \
        M1(1, 0, c, 1); RW1(1, 0, c, 3); RX1(c, 3); ring_piece<c + 3, 3>(rg); SBAR(); \
        M1(1, 1, c, 1); RW1(1, 1, c, 3); H3; SBAR(); \
        M1(0, 0, c, 2); H4; SBAR(); M1(0, 1, c, 2); H5; SBAR(); \
        wait_bar<vm_wait(c)>(); \
        M1(1, 0, c, 3); \
        if constexpr (c < 9) { RW1(0, 0, n_, 0); RX1(n_, 0); RW1(0, 1, n_, 0); RW1(1, 0, n_, 1); RX1(n_, 1); } \
        H6; SBAR(); \
        M1(1, 1, c, 3); \
        if constexpr (c < 9) { RW1(1, 1, n_, 1); } \
        if constexpr (c < 8) ring_piece<c + 4, 0>(rg); \
        SBAR(); } while (0)
        A1C(0, , , ({ rs = dot_ones(xf[0], rs); rq = dot_self(xf[0], rq); rs = dot_ones(xf[1], rs); rq = dot_self(xf[1], rq);
                      cs0 = dot_ones(fw[0][0], cs0); cs1 = dot_ones(fw[0][1], cs1); }), );
        STAMPP(2, 3);
        A1C(1, , , ({ rs = dot_ones(xf[4], rs); rq = dot_self(xf[4], rq); rs = dot_ones(xf[5], rs); rq = dot_self(xf[5], rq);
                      cs0 = dot_ones(fw[0][0], cs0, col2); cs1 = dot_ones(fw[0][1], cs1, col2); }), );
        STAMPP(2, 4);
        A1C(2, , ({ cs0 = swap_add(cs0); cs1 = swap_add(cs1); part[pw_col] = hh ? cs1 : cs0; }),
            ({ float r2 = dot_ones(xf[8], rs), q2 = dot_self(xf[8], rq); r2 = dot_ones(xf[9], r2); q2 = dot_self(xf[9], q2);
               part[pw_row] = (ji & 1) ? rs : r2; part[pw_row + 128] = (ji & 1) ? rq : q2; }),
            ({ pv[0] = part[pr_i]; pv[1] = part[384 + pr_i]; pv[2] = part[768 + pr_i]; pv[3] = part[1152 + pr_i]; }));
        STAMPP(2, 5);
        A1C(3, , __builtin_amdgcn_raw_buffer_store_b64((u32x2){__float_as_uint((pv[0] + pv[1]) + (pv[2] + pv[3])), tag32}, wsr, pub_o, 0, SC1), , );
        STAMPP(2, 6); A1C(4, , , , ); STAMPP(2, 7); A1C(5, , , , ); STAMPP(2, 8);
        A1C(6, , , , ({ gr[0] = ld2_async(ws, gao); gr[1] = ld2_async(ws, gao + gas); gr[2] = ld2_async(ws, gao + 2 * gas); gr[3] = ld2_async(ws, gao + 3 * gas); }));
        STAMPP(2, 9); A1C(7, , , , ); STAMPP(2, 10);
        float rstd, st0, st1;
        unsigned nmu; u32x2 cf[3];
        A1C(8, ({ asm volatile("s_waitcnt vmcnt(6)" : "+v"(gr[0]), "+v"(gr[1]), "+v"(gr[2]), "+v"(gr[3]) :: "memory");
                  st0 = gran_sum(ws, gao, gas, gr, tag32); }),
            ({ u32x2 t = split3(st0); if (w < 4) t[0] = __float_as_uint(st0);
               fin[(w < 6 ? 64 * w : 64 * w + 64) + lane] = t; }), ,
            ({ st0 = *(const LAS float*)(fin + xrow); st1 = *(const LAS float*)(fin + 128 + xrow);
               cf[0] = fin[256 + 192 * hh + 64 * wn + r32]; cf[1] = fin[256 + 192 * hh + 64 * wn + 32 + r32]; }));
        A1C(9, ({ const float mean = st0 * (1.f / 640.f), var = fmaxf(st1 * (1.f / 640.f) - mean * mean, 0.f);
                  nmu = pk2(-mean, -mean);
                  rstd = __builtin_amdgcn_rcpf(bfr(__builtin_amdgcn_sqrtf(bfr(bfr(var) + 1e-5f))));
                  asm volatile("" : "+v"(nmu), "+v"(rstd)); }),
            , ({ acc[0] = MFMA8(cf[0], ((u32x2){nmu, nmu}), acc[0]); acc[1] = MFMA8(cf[1], ((u32x2){nmu, nmu}), acc[1]); }), );
        asm volatile("" : "+{v[64:67]}"(xf[28]), "+{v[68:71]}"(xf[29]), "+{v[72:75]}"(xf[30]), "+{v[76:79]}"(xf[31]), "+{v[80:83]}"(xf[32]), "+{v[84:87]}"(xf[33]),
                          "+{v[88:91]}"(xf[34]), "+{v[92:95]}"(xf[35]), "+{v[96:99]}"(xf[36]), "+{v[100:103]}"(xf[37]), "+{v[104:107]}"(xf[38]), "+{v[108:111]}"(xf[39]));
        STAMPP(2, 11);
        STAMPP(1, 1);
        { const int tile = 4 * m + wm;
          const float sc = j < 5 ? rstd * QSCALE : rstd;
          u32x4 y[4];
#pragma unroll
          for (int i = 0; i < 4; ++i)
#pragma unroll
              for (int e = 0; e < 4; ++e) y[i][e] = pk2(sc * acc[i >> 1][8 * (i & 1) + 2 * e], sc * acc[i >> 1][8 * (i & 1) + 2 * e + 1]);
          if (j < 10) {
              const unsigned base = (j < 5 ? WS_Q + tile * 2560 : WS_KV + tile * KVS) + r32 * 16, ps = j < 5 ? 64 * 2560 : 64 * KVS;
              const int c8 = (j < 5 ? j : j - 5) * 16 + 8 * wn + hh;
#pragma unroll
              for (int i = 0; i < 4; ++i) {
                  const int ci = c8 + 2 * i, pr = ci / 5, pl = ci - 5 * pr;
                  __builtin_amdgcn_raw_buffer_store_b128(y[i], wsr, base + pr * ps + pl * 512, 0, SC1);
              }
          } else {
              LAS char* tp = lds + L_TR + w * 2048;
              const int cu = 2 * (g4 & 1) + (p4 >> 1);
#pragma unroll
              for (int b = 0; b < 2; ++b) {
#pragma unroll
                  for (int v = 0; v < 2; ++v) *(LAS u32x4*)(tp + r32 * 64 + (((2 * v + hh) ^ ((r32 >> 1) & 3)) << 4)) = y[2 * b + v];
                  asm volatile("s_waitcnt lgkmcnt(0)" ::: "memory");
                  const int col = (j - 10) * 128 + 64 * wn + 32 * b + r32, head = col / 20, d = col - 20 * head;
                  const unsigned base = j < 15 ? WS_KV + ((head >> 1) * 64 + tile) * KVS + 2560 + (head & 1) * 1280 + d * 16 + hh * 320 : OOB;
#pragma unroll
                  for (int i = 0; i < 2; ++i) {
                      const int r0 = 16 * i + 8 * hh + q4;
                      const bf16x8 v = ldstr(tp + r0 * 64 + ((cu ^ (q4 >> 1)) << 4) + (p4 & 1) * 8,
                                             tp + (r0 + 4) * 64 + ((cu ^ (2 + (q4 >> 1))) << 4) + (p4 & 1) * 8);
                      __builtin_amdgcn_raw_buffer_store_b128(__builtin_bit_cast(u32x4, v), wsr, base + i * 640, 0, SC1);
                  }
                  asm volatile("s_waitcnt lgkmcnt(0)" ::: "memory");
              }
          } }
        STAMPP(1, 2); STAMPP(4, 1);
        unsigned ga[4];
#pragma unroll
        for (int ks = 0; ks < 4; ++ks) ga[ks] = (g4 & 1) * (12288 + 32) + (8 * (g4 >> 1) + q4) * 192 + wn * 96 + p4 * 8 + (ks ^ rot) * 3072;
        float cs[3] = {0.f, 0.f, 0.f};
        f32x16 ac2[3] = {};
#define RW2(f, b, c, k) do { if constexpr (b == 0) FLIP(c, ga[k]); fw[f][b] = ldstr(lds + ((c) & 1) * 32768 + ga[k] + b * 32, lds + ((c) & 1) * 32768 + ga[k] + b * 32 + 768); } while (0)
#define M2(f, b, c, k) ac2[b] = MFMA32(fw[f][b], xf[4 * ((c) - 10) + (k)], ac2[b]); SBAR()
        SBAR(); wait_bar<3 + 4>();
        RW2(0, 0, 10, 0); RW2(0, 1, 10, 0); RW2(0, 2, 10, 0); RW2(1, 0, 10, 1); RW2(1, 1, 10, 1); RW2(1, 2, 10, 1); SBAR();
#define A2C(c, H0, H1, H2, H3, H5, H6) do { \
        M2(0, 0, c, 0); RW2(0, 0, c, 2); H0; SBAR(); \
        M2(0, 1, c, 0); RW2(0, 1, c, 2); H1; SBAR(); \
        M2(0, 2, c, 0); RW2(0, 2, c, 2); H2; SBAR(); \
        M2(1, 0, c, 1); RW2(1, 0, c, 3); H3; SBAR(); \
        M2(1, 1, c, 1); RW2(1, 1, c, 3); SBAR(); \
        M2(1, 2, c, 1); RW2(1, 2, c, 3); SBAR(); \
        M2(0, 0, c, 2); M2(0, 1, c, 2); H5; SBAR(); \
        if constexpr (c < 19) wait_bar<vm_wait(c)>(); \
        M2(0, 2, c, 2); \
        if constexpr (c < 19) { RW2(0, 0, c + 1, 0); RW2(0, 1, c + 1, 0); RW2(0, 2, c + 1, 0); } \
        H6; SBAR(); \
        M2(1, 0, c, 3); if constexpr (c < 19) RW2(1, 0, c + 1, 1); ring_piece<c + 4, 0>(rg); SBAR(); \
        M2(1, 1, c, 3); if constexpr (c < 19) RW2(1, 1, c + 1, 1); ring_piece<c + 4, 1>(rg); SBAR(); \
        M2(1, 2, c, 3); if constexpr (c < 19) RW2(1, 2, c + 1, 1); ring_piece<c + 4, 2>(rg); SBAR(); STAMPP(4, c - 8); } while (0)
        A2C(10, ring_issue<12>(rg), , ring_issue<13>(rg), , ({ cs[0] = dot_ones(fw[0][0], cs[0]); cs[1] = dot_ones(fw[0][1], cs[1]); cs[2] = dot_ones(fw[0][2], cs[2]); }), );
        A2C(11, , , , , ({ cs[0] = dot_ones(fw[0][0], cs[0], col2); cs[1] = dot_ones(fw[0][1], cs[1], col2); cs[2] = dot_ones(fw[0][2], cs[2], col2);
                           cs[0] = swap_add(cs[0]); cs[1] = swap_add(cs[1]); cs[2] = swap_add(cs[2]);
                           LANE_IDS; part[wm * 384 + 96 * wn + lane] = hh ? cs[1] : cs[0]; part[wm * 384 + 96 * wn + 64 + r32] = cs[2]; }),
            ({ const int pi = (w < 3 ? 64 * w : 0) + lane_id(); pv[0] = part[pi]; pv[1] = part[384 + pi]; pv[2] = part[768 + pi]; pv[3] = part[1152 + pi]; }));
        if (w == 0 && lane_id() == 0) flag_store(ws + WS_FA + bid * 8, tag);
        A2C(12, __builtin_amdgcn_raw_buffer_store_b64((u32x2){__float_as_uint((pv[0] + pv[1]) + (pv[2] + pv[3])), tag32}, wsr, lane_id() * 8 + (w < 3 ? WS_GB + bid * 1536 + w * 512 : OOB), 0, SC1), , , , , );
        A2C(13, , , , , , );
        A2C(14, , , , , , );
        { LANE_IDS;
        const int gbh = w >= 3 && w < 6;
        const unsigned gbo = WS_GB + (xcd + 64 * ji + 32 * gbh) * 1536 + (w < 6 ? w - 3 * gbh : 0) * 512 + lane * 8, fao = WS_FA + (tid & 255) * 8;
        A2C(15, , , , , , ({ gr[0] = ld2_async(ws, gbo); gr[1] = ld2_async(ws, gbo + 8 * 1536); gr[2] = ld2_async(ws, gbo + 16 * 1536); gr[3] = ld2_async(ws, gbo + 24 * 1536);
                   fa = ld2_async(ws, fao); }));
        A2C(16, , , , , , );
        A2C(17, ({ asm volatile("s_waitcnt vmcnt(3)" : "+v"(gr[0]), "+v"(gr[1]), "+v"(gr[2]), "+v"(gr[3]), "+v"(fa) :: "memory");
                   st0 = gran_sum(ws, gbo, 8 * 1536, gr, tag32); }),
            flag_wait(ws, fao, fa, tag),
            fin[256 + (w < 6 ? tid : lane)] = split3(st0),
            ({ const unsigned ring = (unsigned)(size_t)lds + (kh ? L_KV1 : L_WO), vs = 2560u + odd * 1280 + lane * 16;
               at.lb = ring + qq * KVT;
               at.ss = (u32x4){kvoff + qq * KVS, 0u, 0u, 0u};
               at.con = (u32x8){0xddddu, odd * 1024u + lane * 16, hh ? vs - 1536 : odd * 1024u + lane * 16, vs - 1536, (unsigned)(size_t)lds + L_FLAG + 12 * kh, 1u, 0u, 0u};
               asm volatile("" : "+{v[204:211]}"(at.con)); }), ,
            ({ cf[0] = fin[256 + 192 * hh + 96 * wn + r32]; cf[1] = fin[256 + 192 * hh + 96 * wn + 32 + r32]; cf[2] = fin[256 + 192 * hh + 96 * wn + 64 + r32]; }));
        A2C(18, attn_fetch(at), , attn_load_q(at, qoff, odd * 1536 + lane * 16, hh ? OOB : 1024 + r32 * 16 + odd * 8),
            ({ ac2[0] = MFMA8(cf[0], ((u32x2){nmu, nmu}), ac2[0]); ac2[1] = MFMA8(cf[1], ((u32x2){nmu, nmu}), ac2[1]); ac2[2] = MFMA8(cf[2], ((u32x2){nmu, nmu}), ac2[2]); }), , attn_init_o<0>(at));
        A2C(19, attn_fetch(at), attn_init_o<1>(at),
            ({
               const unsigned ring = (unsigned)(size_t)lds + (kh ? L_KV1 : L_WO), va = ring + 1536 + hh * 320 + r32 * 16, vx = ring + (r32 == 20 ? 2816 : 2832);
               at.kv[28] = ring + odd * 512 + lane * 16; at.kv[29] = ring + (hh ? 2816 : (odd ? 8 : 1024) + r32 * 16);
               at.kv[30] = r32 < 20 ? va : vx; at.kv[31] = r32 < 20 ? va + 640 : vx;
               asm volatile("" : "+{v[160:191]}"(at.kv)); }),
            attn_init_o<2>(at), ,
            ({ attn_init_o<3>(at);
               if (lane < 24) *(LAS unsigned*)(lds + (kh ? L_KV1 : L_WO) + ((lane >> 3) * 4 + qq) * KVT + 2816 + (lane & 7) * 4) = (lane & 4) ? 0u : 0x3f803f80u; }));
        STAMPP(1, 3);
        __builtin_amdgcn_s_setprio(3);
        attn_begin<3>(at);
        { const unsigned base = WS_HP + (8 * m + 2 * wm + (r32 >> 4)) * (272 * 256) + (2 * (6 * j + 3 * wn) + hh) * 256 + (r32 & 15) * 16;
          const float r2 = rstd * rstd;
#pragma unroll
          for (int b = 0; b < 3; ++b) {
              u32x4 v;
#pragma unroll
              for (int e = 0; e < 4; ++e) v[e] = pk2(ac2[b][2 * e] * r2 * ac2[b][8 + 2 * e], ac2[b][2 * e + 1] * r2 * ac2[b][9 + 2 * e]);
              __builtin_amdgcn_raw_buffer_store_b128(v, wsr, base + b * 512, 0, SC1);
          } }
        STAMPP(1, 4); STAMPP(3, 1);
        c[0] = max16<0>(at.sa); c[1] = max16<16>(at.sa);
        attn_set_reference(at, c, hh);
        asm volatile("" : "+v"(c[0]), "+v"(c[1]));
        attn_scores0(at);
        }
    }
    u32x32 pfa, pfb; u32x8 pfc;
    const int rb = 4 * xcd + (rr >> 3), ct = rr & 7;
    const unsigned aob = WS_HP + 4 * rb * (272 * 256), bob = WS_WP + 5 * ct * (68 * 1024);
    const int krot = 2 * ((ct + ((3 * (rr >> 3) + 1) >> 1)) % 6);
    {
        LANE_IDS;
        const int unit = bid + 256 * w, us = unit / 10, nb = unit - 10 * us;
        const char* psrc = us < 48 ? wd : wo;
        const u32x4 psrd = {(unsigned)(size_t)psrc, (unsigned)((size_t)psrc >> 32) & 0xffffu, unit < 680 ? (us < 48 ? 1536u * 1280u : 640u * 1280u) : 0u, 0x00020000u};
        const unsigned plds = L_TR + min(w, 3) * 4096, pvo = (hh * (us < 64 ? 8 : 20) + ((lane >> 3) & 3)) * 1280 + (lane & 7) * 16;
        { const int ra = us < 48 ? 32 * us : (us < 64 ? 40 * (us - 48) : 160 * (us - 64) + 16), r1 = us < 64 ? 4 : 40, r2 = us < 48 ? 16 : (us < 64 ? 20 : 80);
#pragma unroll
          for (int i = 0; i < 4; ++i)
              asm volatile("s_mov_b32 m0, %3\n\ts_nop 0\n\tbuffer_load_dwordx4 %0, %1, %2 offen lds"
                           :: "v"(pvo), "s"(psrd), "s"((ra + (i & 1) * r1 + (i >> 1) * r2) * 1280 + nb * 128), "s"((unsigned)(size_t)lds + plds + i * 1024) : "memory"); }
        STAMPP(1, 5); STAMPP(3, 2);
        attn_iter_first<7>(at); STAMPP(3, 3);
        if (kh) {
            asm volatile("s_waitcnt vmcnt(3)" ::: "memory");
            arrive_publish(lflag + 1, 7u, lane, ws + WS_FH + bid * 8, tag);
        }
        asm volatile("s_setprio 2\n\ts_cmp_eq_u32 %0, 0\n\ts_cbranch_scc1 1f\n\ts_setprio 1\n\t1:" :: "s"(kh) : "scc");
        attn_iter<0>(at); STAMPP(3, 4);
        { const unsigned pdst = lane * 16 + (unit < 680 ? WS_WP + (4 * nb * 68 + us) * 1024 : OOB);
          const LAS char* prd = lds + plds + (8 * (lane >> 4) + ((lane >> 2) & 3)) * 128 + (lane & 3) * 8;
#pragma unroll
          for (int i = 0; i < 4; ++i)
              __builtin_amdgcn_raw_buffer_store_b128(__builtin_bit_cast(u32x4, ldstr(prd + 32 * i, prd + 512 + 32 * i)), wsr, pdst + i * (68 * 1024), 0, SC1); }
        attn_iter<4>(at);
        if (!kh) arrive_publish(lflag + 1, 7u, lane, ws + WS_FH + bid * 8, tag);
        const int ntiles = kh ? 64 - NT0 : NT0;
        attn_loop(at, ntiles / 4 - 6); STAMPP(3, 5);
        u32x4 fh0 = ld4_async(ws, WS_FH + lane * 16), fh1 = ld4_async(ws, WS_FH + 1024 + lane * 16);
        attn_iter<2>(at); STAMPP(3, 6);
        asm volatile("" : "+v"(fh0), "+v"(fh1));
        if (!kh) {
            flag_wait4(ws, WS_FH + lane * 16, fh0, fh1, tag);
            if (bid == 0 && tid == 0) flag_store(ws + WS_EPOCH, tag);
        }
        STAMPP(3, 7);
        attn_last(at, kh, pfa, pfb, pfc, aob + (12 * w + krot) * 1024, bob + (12 * w + krot) * 1024, lane * 16);
        attn_check_redo(at, c, wsr, kvoff, odd, ntiles);
        STAMPP(1, 6); STAMPP(3, 8);
    }
    u32x2 xv[3]; int xoff[3];
    auto load_x = [&](int lane) {
#pragma unroll
        for (int it = 0; it < 3; ++it) {
            const int tl = min(w + 8 * it, 19), i = tl / 5, n = tl - 5 * i;
            xoff[it] = (64 * rb + 16 * i + (lane & 15)) * 1280 + (80 * ct + 16 * n + 4 * (lane >> 4)) * 2;
            xv[it] = *(const GAS u32x2*)(x + xoff[it]);
        }
    };
    if (kh == 0) {
        __builtin_amdgcn_s_setprio(0);
        LANE_IDS;
        { LAS char* snd = lds + L_MRG + w * 6656;
#pragma unroll
          for (int i = 0; i < 6; ++i) { const int o = 16 * (i / 3) + 4 * (i % 3); *(LAS f32x4*)(snd + i * 1024 + lane * 16) = (f32x4){at.oo[o], at.oo[o + 1], at.oo[o + 2], at.oo[o + 3]}; }
          *(LAS u32x2*)(snd + 6144 + lane * 8) = (u32x2){__float_as_uint(c[0]), __float_as_uint(c[1])};
          asm volatile("s_waitcnt lgkmcnt(0)" ::: "memory");
          if (lane == 0) __hip_atomic_store(lflag + 4 + w, 1u, __ATOMIC_RELAXED, __HIP_MEMORY_SCOPE_WORKGROUP); }
        STAMPP(1, 7); STAMPP(3, 9);
        f32x4 acc[4][5] = {};
        u32x4 a0[4], b0[5], a1[4], b1[5];
        asm volatile("s_waitcnt vmcnt(0)" : "+{v[0:31]}"(pfa), "+{v[32:63]}"(pfb), "+{v[212:219]}"(pfc) :: "memory");
#pragma unroll
        for (int i = 0; i < 4; ++i) {
            a0[i] = (u32x4){pfa[4 * i], pfa[4 * i + 1], pfa[4 * i + 2], pfa[4 * i + 3]}; a1[i] = (u32x4){pfa[16 + 4 * i], pfa[17 + 4 * i], pfa[18 + 4 * i], pfa[19 + 4 * i]};
            b0[i] = (u32x4){pfb[4 * i], pfb[4 * i + 1], pfb[4 * i + 2], pfb[4 * i + 3]}; b1[i] = (u32x4){pfb[16 + 4 * i], pfb[17 + 4 * i], pfb[18 + 4 * i], pfb[19 + 4 * i]};
        }
        b0[4] = (u32x4){pfc[0], pfc[1], pfc[2], pfc[3]}; b1[4] = (u32x4){pfc[4], pfc[5], pfc[6], pfc[7]};
        auto loada = [&](int s, u32x4 (&a)[4]) {
#pragma unroll
            for (int i = 0; i < 4; ++i) a[i] = __builtin_amdgcn_raw_buffer_load_b128(wsr, lane * 16, aob + s * 1024 + i * (272 * 256), SC1);
        };
        auto loadb = [&](int s, u32x4 (&b)[5]) {
#pragma unroll
            for (int i = 0; i < 5; ++i) b[i] = __builtin_amdgcn_raw_buffer_load_b128(wsr, lane * 16, bob + s * 1024 + i * (68 * 1024), SC1);
        };
        auto mma = [&](const u32x4 (&a)[4], const u32x4 (&b)[5]) {
#pragma unroll
            for (int i = 0; i < 4; ++i)
#pragma unroll
                for (int n = 0; n < 5; ++n) acc[i][n] = MFMA16(__builtin_bit_cast(bf16x8, b[n]), __builtin_bit_cast(bf16x8, a[i]), acc[i][n]);
        };
        u32x4 a2[4], b2[5], a3[4], b3[5];
        auto kst = [&](int i) { const int k = krot + i; return 12 * w + (k >= 12 ? k - 12 : k); };
        const unsigned wl = (unsigned)(size_t)lds + L_WO + w * 25600;
        auto stage = [&](int n) {
#pragma unroll
            for (int s = 0; s < 5; ++s) glds16_dev(lane * 16, ws + bob + n * (68 * 1024) + (48 + 5 * w + s) * 1024, wl + (n * 5 + s) * 1024);
        };
        loada(kst(2), a2); loadb(kst(2), b2); loada(kst(3), a3); loadb(kst(3), b3); SBAR();
        mma(a0, b0); SBAR(); loada(kst(4), a0); loadb(kst(4), b0); SBAR();
#pragma unroll 1
        for (unsigned spin = 0; spin < SPIN_CAP; ++spin) {
            if (__all(__hip_atomic_load(lflag + 4 + (lane & 3), __ATOMIC_RELAXED, __HIP_MEMORY_SCOPE_WORKGROUP) != 0u)) break;
            __builtin_amdgcn_s_sleep(1);
        }
        mma(a1, b1); SBAR(); stage(0); loada(kst(5), a1); loadb(kst(5), b1); SBAR();
        mma(a2, b2); SBAR(); stage(1); loada(kst(6), a2); loadb(kst(6), b2); SBAR();
        mma(a3, b3); SBAR(); stage(2); loada(kst(7), a3); loadb(kst(7), b3); SBAR();
        mma(a0, b0); SBAR(); stage(3); loada(kst(8), a0); loadb(kst(8), b0); SBAR();
        mma(a1, b1); SBAR(); stage(4); loada(kst(9), a1); loadb(kst(9), b1); SBAR();
        mma(a2, b2); SBAR(); loada(kst(10), a2); loadb(kst(10), b2); SBAR();
        mma(a3, b3); SBAR(); loada(kst(11), a3); loadb(kst(11), b3); SBAR();
        mma(a0, b0); SBAR(); mma(a1, b1); SBAR(); mma(a2, b2); SBAR(); mma(a3, b3); SBAR();
        load_x(lane);
        STAMPP(1, 8); STAMPP(3, 10);
        flag_poll(ws, WS_FO + ((rb >> 2) * 32 + r32) * 128, tag);
        STAMPP(1, 9);
        __builtin_amdgcn_s_setprio(3);
        u32x4 a4[4];
        loada(48 + 5 * w, a0); loada(49 + 5 * w, a1); loada(50 + 5 * w, a2); loada(51 + 5 * w, a3); loada(52 + 5 * w, a4); SBAR();
        auto ldsb = [&](int s, u32x4 (&b)[5]) {
#pragma unroll
            for (int n = 0; n < 5; ++n) b[n] = *(const LAS u32x4*)(lds + L_WO + w * 25600 + (n * 5 + s) * 1024 + lane * 16);
        };
        ldsb(0, b0); ldsb(1, b1); ldsb(2, b2); SBAR();
        mma(a0, b0); SBAR(); ldsb(3, b0); SBAR(); mma(a1, b1); SBAR(); ldsb(4, b1); SBAR(); mma(a2, b2); mma(a3, b0); mma(a4, b1);
        STAMPP(1, 10);
        LAS f32x4* red = (LAS f32x4*)(lds + L_WO + w * 25600);
#pragma unroll
        for (int i = 0; i < 4; ++i)
#pragma unroll
            for (int n = 0; n < 5; ++n) red[(i * 5 + n) * 64 + lane] = acc[i][n];
    } else {
        LANE_IDS;
#pragma unroll 1
        for (unsigned spin = 0; spin < SPIN_CAP; ++spin) {
            if (__builtin_amdgcn_readfirstlane(__hip_atomic_load(lflag + w, __ATOMIC_RELAXED, __HIP_MEMORY_SCOPE_WORKGROUP))) break;
            __builtin_amdgcn_s_sleep(1);
        }
        asm volatile("" ::: "memory");
        const LAS char* rcv = lds + L_MRG + qq * 6656;
        const u32x2 cp = *(const LAS u32x2*)(rcv + 6144 + lane * 8);
        f32x4 r[6];
#pragma unroll
        for (int i = 0; i < 6; ++i) r[i] = *(const LAS f32x4*)(rcv + i * 1024 + lane * 16);
        const unsigned base = WS_HP + (16 * mq + 4 * qq + (r32 >> 4)) * (272 * 256) + (r32 & 15) * 16;
        const unsigned om = base + (192 + 2 * g + hh) * 256, orr = hh ? OOB : base + (256 + (g >> 1)) * 256 + (g & 1) * 8;
#pragma unroll
        for (int t = 0; t < 2; ++t) {
            const float co = __uint_as_float(cp[t]), cx = fmaxf(c[t], co);
            float wa = __builtin_amdgcn_exp2f(c[t] - cx), wb = __builtin_amdgcn_exp2f(co - cx);
            const float inv = __builtin_amdgcn_rcpf(swap_add(hh ? at.oo[16 * t + 8] * wa + r[3 * t + 2][0] * wb : 0.f));
            wa *= inv; wb *= inv;
            float f[12];
#pragma unroll
            for (int i = 0; i < 12; ++i) f[i] = at.oo[16 * t + i] * wa + r[3 * t + (i >> 2)][i & 3] * wb;
            __builtin_amdgcn_raw_buffer_store_b128((u32x4){pk2(f[0], f[1]), pk2(f[2], f[3]), pk2(f[4], f[5]), pk2(f[6], f[7])}, wsr, om + t * (2 * 272 * 256), 0, SC1);
            __builtin_amdgcn_raw_buffer_store_b64((u32x2){pk2(f[8], f[9]), pk2(f[10], f[11])}, wsr, orr + t * (2 * 272 * 256), 0, SC1);
        }
        STAMPP(1, 7); STAMPP(3, 9);
        asm volatile("s_waitcnt vmcnt(0)" ::: "memory");
        arrive_publish(lflag + 2, 3u, lane, ws + WS_FO + (mq * 32 + g) * 128, tag);
        STAMPP(1, 8); STAMPP(3, 10);
        load_x(lane);
        STAMPP(1, 9); STAMPP(1, 10);
    }
    lds_bar();
    { LANE_IDS;
      const LAS f32x4* red = (const LAS f32x4*)(lds + L_WO);
#pragma unroll
      for (int it = 0; it < 3; ++it) {
          if (it < 2 || w < 4) {
              const int item = tid + 512 * it;
              const f32x4 s = (red[item] + red[1600 + item]) + (red[3200 + item] + red[4800 + item]);
              u32x2 v;
              v[0] = pk2(s[0] + __uint_as_float(xv[it][0] << 16), s[1] + __uint_as_float(xv[it][0] & 0xffff0000u));
              v[1] = pk2(s[2] + __uint_as_float(xv[it][1] << 16), s[3] + __uint_as_float(xv[it][1] & 0xffff0000u));
              *(GAS u32x2*)(out + xoff[it]) = v;
          }
      } }
    STAMPP(1, 11); STAMPP(3, 11);
    STAMP_OUT;
}

extern "C" void kernel_launch(void* const* d_in, const int* in_sizes, int n_in, void* d_out, int out_size, void* d_ws, size_t ws_size,
                              hipStream_t stream) {
    fused_block<<<256, 512, 0, stream>>>((const char*)d_in[0], (const char*)d_in[1], (const char*)d_in[2], (const char*)d_in[4],
                                         (const char*)d_in[5], (const char*)d_in[6], (char*)d_out, (char*)d_ws);
}
```

```cpp
#define STAMPS 1
#define DIAG_A 0
#define DIAG_B 0
#define DIAG_W 255
#include <hip/hip_runtime.h>
#include <hip/hip_bf16.h>

typedef __bf16 bf16x8 __attribute__((ext_vector_type(8)));
typedef __bf16 bf16x2 __attribute__((ext_vector_type(2)));
typedef short s16x4 __attribute__((ext_vector_type(4)));
typedef float f32x32 __attribute__((ext_vector_type(32)));
typedef float f32x16 __attribute__((ext_vector_type(16)));
typedef float f32x4 __attribute__((ext_vector_type(4)));
typedef unsigned u32x32 __attribute__((ext_vector_type(32)));
typedef unsigned u32x16 __attribute__((ext_vector_type(16)));
typedef unsigned u32x8 __attribute__((ext_vector_type(8)));
typedef unsigned u32x4 __attribute__((ext_vector_type(4)));
typedef unsigned u32x2 __attribute__((ext_vector_type(2)));
typedef unsigned long long u64;
#define LAS __attribute__((address_space(3)))
#define GAS __attribute__((address_space(1)))
#define DEV __device__ __forceinline__

constexpr unsigned WS_EPOCH = 0;
constexpr unsigned WS_FA = 4096;
constexpr unsigned WS_FH = 8192;
constexpr unsigned WS_FO = 16384;
constexpr unsigned WS_GA = 65536;
constexpr unsigned WS_GB = WS_GA + 256 * 384 * 8;
constexpr unsigned WS_Q = WS_GB + 256 * 192 * 8;
constexpr unsigned KVS = 5120;
constexpr unsigned WS_KV = WS_Q + 16 * 64 * 2560;
constexpr unsigned WS_HP = WS_KV + 16 * 64 * KVS;
constexpr unsigned WS_WP = WS_HP + 128 * 272 * 256;
constexpr unsigned WS_END = WS_WP + 40 * 68 * 1024;
constexpr unsigned WS_STAMP = 32u << 20;
constexpr unsigned OOB = 0x40000000u;

constexpr int L_TR = 131072;
constexpr int L_PART = L_TR + 16384;
constexpr int L_FIN = L_PART + 6144;
constexpr int KVT = 2848;
constexpr int L_KV1 = 0;
constexpr int L_KV0 = 12 * KVT;
constexpr int L_RED2 = L_KV0 + 81920;
constexpr int L_MRG = 136576;
constexpr int L_FLAG = L_MRG + 26624;
constexpr int L_TOTAL = 163840;

constexpr float QSCALE = 0.22360679774997896f * 1.4426950408889634f;
constexpr int SC1 = 16;
#define NT0 32
#define CODE_LINES 8
constexpr unsigned SPIN_CAP = 1u << 22;

DEV int lane_id() {
    int l; asm volatile("v_mbcnt_lo_u32_b32 %0, -1, 0\n\tv_mbcnt_hi_u32_b32 %0, -1, %0" : "=v"(l)); return l;
}
DEV unsigned pk2(float lo, float hi) { bf16x2 v = {(__bf16)lo, (__bf16)hi}; return __builtin_bit_cast(unsigned, v); }
DEV float bfr(float x) { return (float)(__bf16)x; }
DEV u32x2 split3(float v) {
    const float a = bfr(v), b = bfr(v - a), c = v - a - b;
    return (u32x2){pk2(a, b), pk2(c, 0.f)};
}
DEV float swap_add(float v) {
    auto r = __builtin_amdgcn_permlane32_swap(__float_as_uint(v), __float_as_uint(v), false, false);
    return __uint_as_float(r[0]) + __uint_as_float(r[1]);
}
DEV float swap_max(float v) {
    auto r = __builtin_amdgcn_permlane32_swap(__float_as_uint(v), __float_as_uint(v), false, false);
    return fmaxf(__uint_as_float(r[0]), __uint_as_float(r[1]));
}
DEV void glds16(unsigned voff, const void* sbase, unsigned ldsdst) {
    asm volatile("s_mov_b32 m0, %2\n\ts_nop 0\n\tglobal_load_lds_dwordx4 %0, %1" ::"v"(voff), "s"(sbase), "s"(ldsdst) : "memory");
}
DEV void glds4(unsigned voff, const void* sbase, unsigned ldsdst) {
    asm volatile("s_mov_b32 m0, %2\n\ts_nop 0\n\tglobal_load_lds_dword %0, %1" ::"v"(voff), "s"(sbase), "s"(ldsdst) : "memory");
}
template <int N> DEV void wait_bar() { asm volatile("s_waitcnt vmcnt(%0) lgkmcnt(0)\n\ts_barrier" ::"i"(N) : "memory"); }
DEV void lds_bar() { asm volatile("s_waitcnt lgkmcnt(0)\n\ts_barrier" ::: "memory"); }
DEV bf16x8 lds128(const LAS char* p) { return *(const LAS bf16x8*)p; }
DEV bf16x8 ldstr(const LAS char* p0, const LAS char* p1) {
    s16x4 a = __builtin_amdgcn_ds_read_tr16_b64_v4i16((LAS s16x4*)p0), b = __builtin_amdgcn_ds_read_tr16_b64_v4i16((LAS s16x4*)p1);
    return __builtin_bit_cast(bf16x8, __builtin_shufflevector(a, b, 0, 1, 2, 3, 4, 5, 6, 7));
}
DEV float dot_ones(bf16x8 v, float acc, unsigned ones = 0x3f803f80u) {
    const bf16x2 one = __builtin_bit_cast(bf16x2, ones);
#pragma unroll
    for (int i = 0; i < 4; ++i) acc = __builtin_amdgcn_fdot2_f32_bf16((bf16x2){v[2 * i], v[2 * i + 1]}, one, acc, false);
    return acc;
}
DEV float dot_self(bf16x8 v, float acc) {
#pragma unroll
    for (int i = 0; i < 4; ++i) { bf16x2 p = {v[2 * i], v[2 * i + 1]}; acc = __builtin_amdgcn_fdot2_f32_bf16(p, p, acc, false); }
    return acc;
}
#define MFMA32(a, b, c) __builtin_amdgcn_mfma_f32_32x32x16_bf16(a, b, c, 0, 0, 0)
#define MFMA8(a, b, c) __builtin_amdgcn_mfma_f32_32x32x8bf16_1k(__builtin_bit_cast(s16x4, a), __builtin_bit_cast(s16x4, b), c, 0, 0, 0)
#define MFMA16(a, b, c) __builtin_amdgcn_mfma_f32_16x16x32_bf16(a, b, c, 0, 0, 0)
#define SBAR() __builtin_amdgcn_sched_barrier(0)

#define STAMP(i) do { u64 a_, b_; SBAR(); asm volatile("s_memrealtime %0\n\ts_memtime %1\n\ts_waitcnt lgkmcnt(0)" : "=&s"(a_), "=&s"(b_) :: "memory"); \
    st_rt[i] = (unsigned)a_; st_cy[i] = (unsigned)b_; SBAR(); } while (0)
#define STAMPP(p, i) do { if (STAMPS == p) STAMP(i); } while (0)
#define STAMP_DECL unsigned st_rt[12] = {}, st_cy[12] = {}
#define STAMP_OUT do { const unsigned d_ = ((DIAG_W >> w) & 1) ? st_rt[DIAG_B] - st_rt[DIAG_A] : 0u; const unsigned t_ = (unsigned)__builtin_amdgcn_s_memrealtime(); \
    while ((unsigned)__builtin_amdgcn_s_memrealtime() - t_ < d_) __builtin_amdgcn_s_sleep(1); } while (0)

DEV u32x2 ld2_async(const char* base, unsigned off) { u32x2 v; asm volatile("global_load_dwordx2 %0, %1, %2 sc1" : "=v"(v) : "v"(off), "s"(base) : "memory"); return v; }
DEV u32x4 ld4_async(const char* base, unsigned off) { u32x4 v; asm volatile("global_load_dwordx4 %0, %1, %2 sc1" : "=v"(v) : "v"(off), "s"(base) : "memory"); return v; }
DEV void flag_store(char* p, u64 tag) { __hip_atomic_store((GAS u64*)p, tag, __ATOMIC_RELAXED, __HIP_MEMORY_SCOPE_AGENT); }
DEV float gran_sum(const char* ws, unsigned off, unsigned stride, u32x2 (&g)[4], unsigned tag) {
    if (__builtin_expect(__any(((g[0][1] ^ tag) | (g[1][1] ^ tag) | (g[2][1] ^ tag) | (g[3][1] ^ tag)) != 0u), 0)) {
#pragma unroll 1
        for (unsigned spin = 0; spin < SPIN_CAP; ++spin) {
            asm volatile("s_sleep 1\n\tglobal_load_dwordx2 %0, %4, %8 sc1\n\tglobal_load_dwordx2 %1, %5, %8 sc1\n\t"
                         "global_load_dwordx2 %2, %6, %8 sc1\n\tglobal_load_dwordx2 %3, %7, %8 sc1\n\ts_waitcnt vmcnt(0)"
                         : "=&v"(g[0]), "=&v"(g[1]), "=&v"(g[2]), "=&v"(g[3])
                         : "v"(off), "v"(off + stride), "v"(off + 2 * stride), "v"(off + 3 * stride), "s"(ws) : "memory");
            if (__all(g[0][1] == tag && g[1][1] == tag && g[2][1] == tag && g[3][1] == tag)) break;
        }
    }
    return (__uint_as_float(g[0][0]) + __uint_as_float(g[1][0])) + (__uint_as_float(g[2][0]) + __uint_as_float(g[3][0]));
}
template <int SLEEP> DEV void flag_poll(const char* ws, unsigned off, u64 tag) {
#pragma unroll 1
    for (unsigned spin = 0; spin < SPIN_CAP; ++spin) {
        u64 f;
        asm volatile("global_load_dwordx2 %0, %1, %2 sc1\n\ts_waitcnt vmcnt(0)" : "=&v"(f) : "v"(off), "s"(ws) : "memory");
        if (__all(f == tag)) break;
        __builtin_amdgcn_s_sleep(SLEEP);
    }
}
DEV void lds_wait(LAS unsigned* word) {
#pragma unroll 1
    for (unsigned spin = 0; spin < SPIN_CAP; ++spin) {
        if (__builtin_amdgcn_readfirstlane(__hip_atomic_load(word, __ATOMIC_RELAXED, __HIP_MEMORY_SCOPE_WORKGROUP))) break;
        __builtin_amdgcn_s_sleep(2);
    }
    asm volatile("" ::: "memory");
}
DEV void flag_wait(const char* ws, unsigned off, u32x2 f, u64 tag) {
    if (__builtin_expect(__any(__builtin_bit_cast(u64, f) != tag), 0)) flag_poll<8>(ws, off, tag);
}
DEV void flag_wait4(const char* ws, unsigned off, u32x4 f, u32x4 g, u64 tag) {
    const unsigned lo = (unsigned)tag, hi = (unsigned)(tag >> 32);
#pragma unroll 1
    for (unsigned spin = 0; spin < SPIN_CAP; ++spin) {
        if (__all(((f[0] ^ lo) | (f[1] ^ hi) | (f[2] ^ lo) | (f[3] ^ hi) | (g[0] ^ lo) | (g[1] ^ hi) | (g[2] ^ lo) | (g[3] ^ hi)) == 0u)) break;
        asm volatile("s_sleep 32\n\tglobal_load_dwordx4 %0, %2, %3 sc1\n\tglobal_load_dwordx4 %1, %2, %3 offset:1024 sc1\n\ts_waitcnt vmcnt(0)"
                     : "=&v"(f), "=&v"(g) : "v"(off), "s"(ws) : "memory");
    }
}
DEV void arrive_publish(LAS unsigned* cnt, unsigned last, int lane, char* flag, u64 tag) {
    if (lane == 0) {
        const unsigned old = __hip_atomic_fetch_add(cnt, 1u, __ATOMIC_RELAXED, __HIP_MEMORY_SCOPE_WORKGROUP);
        if (old == last) flag_store(flag, tag);
    }
}

#define EXP4(a, b, c, d) "v_exp_f32 v" #a ", v" #a "\n\tv_exp_f32 v" #b ", v" #b "\n\tv_exp_f32 v" #c ", v" #c "\n\tv_exp_f32 v" #d ", v" #d "\n\t"
#define E_S0_0 EXP4(64, 65, 66, 67)
#define E_S0_1 EXP4(68, 69, 70, 71)
#define E_S0_2 EXP4(72, 73, 74, 75)
#define E_S0_3 EXP4(76, 77, 78, 79)
#define E_S1_0 EXP4(80, 81, 82, 83)
#define E_S1_1 EXP4(84, 85, 86, 87)
#define E_S1_2 EXP4(88, 89, 90, 91)
#define E_S1_3 EXP4(92, 93, 94, 95)
#define E_S2_0 EXP4(96, 97, 98, 99)
#define E_S2_1 EXP4(100, 101, 102, 103)
#define E_S2_2 EXP4(104, 105, 106, 107)
#define E_S2_3 EXP4(108, 109, 110, 111)
#define E_S3_0 EXP4(112, 113, 114, 115)
#define E_S3_1 EXP4(116, 117, 118, 119)
#define E_S3_2 EXP4(120, 121, 122, 123)
#define E_S3_3 EXP4(124, 125, 126, 127)
#define R_S0 "v[64:79]"
#define R_S1 "v[80:95]"
#define R_S2 "v[96:111]"
#define R_S3 "v[112:127]"
#define R_S0L "v[64:71]"
#define R_S0H "v[72:79]"
#define R_S1L "v[80:87]"
#define R_S1H "v[88:95]"
#define R_S2L "v[96:103]"
#define R_S2H "v[104:111]"
#define R_S3L "v[112:119]"
#define R_S3H "v[120:127]"
#define R_O0 "v[128:143]"
#define R_O1 "v[144:159]"
#define R_V0A "v[160:163]"
#define R_V0B "v[164:167]"
#define R_V1A "v[168:171]"
#define R_V1B "v[172:175]"
#define R_K0M "v[176:179]"
#define R_K1M "v[180:183]"
#define R_K0R "v[184:185]"
#define R_K1R "v[186:187]"
#define R_Q0M "v[192:195]"
#define R_Q1M "v[196:199]"
#define R_Q0R "v[200:201]"
#define R_Q1R "v[202:203]"
#define QK1(s, k, q) "v_mfma_f32_32x32x16_bf16 " R_S##s ", " R_K##k##M ", " R_Q##q##M ", 0\n\t"
#define QK2(s, k, q) "v_mfma_f32_32x32x8_bf16 " R_S##s ", " R_K##k##R ", " R_Q##q##R ", " R_S##s "\n\t"
#define PV1(o, v, s) "v_smfmac_f32_32x32x32_bf16 " R_O##o ", " R_V##v##A ", " R_S##s##L ", v204\n\t"
#define PV2(o, v, s) "v_smfmac_f32_32x32x32_bf16 " R_O##o ", " R_V##v##B ", " R_S##s##H ", v204\n\t"
#define PERIOD(e, sn, k, q, sp, v, o) \
    QK1(sn, k, q) E_S##e##_0 PV1(o, v, sp) E_S##e##_1 QK2(sn, k, q) E_S##e##_2 PV2(o, v, sp) E_S##e##_3
#define PERIOD_NOQK(e, sp, v, o) E_S##e##_0 PV1(o, v, sp) E_S##e##_1 E_S##e##_2 PV2(o, v, sp) E_S##e##_3
#define PERIOD_NOPV(e, sn, k, q) QK1(sn, k, q) E_S##e##_0 E_S##e##_1 QK2(sn, k, q) E_S##e##_2 E_S##e##_3
#define RDK(k, o) "ds_read_b128 " R_K##k##M ", v188 offset:" #o "\n\tds_read_b64 " R_K##k##R ", v189 offset:" #o "\n\t"
#define RDV(v, o) "ds_read_b128 " R_V##v##A ", v190 offset:" #o "\n\tds_read_b128 " R_V##v##B ", v191 offset:" #o "\n\t"
#define LGK0 "s_waitcnt lgkmcnt(0)\n\t"
#define FETCH "s_add_u32 m0, s62, %[lb]\n\ts_addk_i32 s62, 11392\n\tbuffer_load_dwordx4 v205, %[srd], s60 offen sc1 lds\n\t" \
              "buffer_load_dwordx4 v206, %[srd], s60 offen offset:1024 sc1 lds\n\ts_mov_b32 exec_hi, 0xffff\n\t" \
              "buffer_load_dwordx4 v207, %[srd], s60 offen offset:2048 sc1 lds\n\ts_mov_b32 exec_hi, -1\n\t" \
              "s_addk_i32 s60, 20480\n\ts_cmp_eq_u32 s62, 34176\n\ts_cselect_b32 s62, 0, s62\n\t"
#define NOFETCH "s_addk_i32 s62, 11392\n\ts_cmp_eq_u32 s62, 34176\n\ts_cselect_b32 s62, 0, s62\n\t"
#define ARRIVE(n) "s_waitcnt vmcnt(" n ")\n\ts_mov_b64 exec, 1\n\tds_add_u32 v208, v209\n\ts_mov_b64 exec, -1\n\ts_addk_i32 s61, 4\n\t"
#define PEEK "ds_read_b32 v210, v208\n\t"
#define SYNC(lab) lab ":\n\tv_readfirstlane_b32 s63, v210\n\ts_cmp_ge_u32 s63, s61\n\ts_cbranch_scc1 " lab "1f\n\t" \
                  "s_sleep 1\n\t" PEEK LGK0 "s_branch " lab "b\n\t" lab "1:\n\t" \
                  "s_cmp_eq_u32 s62, 11392\n\ts_cselect_b32 s63, %[dn], %[up]\n\tv_add_u32 v188, s63, v188\n\tv_add_u32 v189, s63, v189\n\t"
#define VNEXT "v_add_u32 v190, s63, v190\n\tv_add_u32 v191, s63, v191\n\t"
#define PE0 PERIOD(0, 2, 1, 0, 3, 1, 1)
#define PE1 PERIOD(1, 3, 1, 1, 0, 0, 0)
#define PE2 PERIOD(2, 0, 0, 0, 1, 0, 1)
#define PE3 PERIOD(3, 1, 0, 1, 2, 1, 0)
#define ITER_X(P0, lab, na, F, e0, e1, e2, e3) \
        LGK0 e0 P0 RDK(0, 5696) RDV(1, 2848) PE1 \
        LGK0 ARRIVE(na) e1 PE2 RDK(1, 8544) RDV(0, 5696) PEEK PE3 \
        LGK0 e2 PE0 SYNC(lab) RDK(0, 0) RDV(1, 8544) F PE1 \
        LGK0 VNEXT e3 PE2 RDK(1, 2848) RDV(0, 0) PE3
#define ITER_LAST(e0, e1, e2, e3) \
        LGK0 e0 PE0 RDK(0, 5696) RDV(1, 2848) PE1 \
        LGK0 e1 PE2 RDK(1, 8544) RDV(0, 5696) PE3 \
        LGK0 e2 PE0 RDV(1, 8544) PE1 \
        LGK0 e3 PERIOD_NOQK(2, 1, 0, 1) PERIOD_NOQK(3, 2, 1, 0) "s_nop 1\n\t" PV1(1, 1, 3) PV2(1, 1, 3) "s_nop 15\n\t"

struct Attn { f32x32 sa, sb, oo; u32x32 kv; u32x8 qm, con; u32x4 qr, srd, ss; unsigned lb; };
#define ATTN_DMA(a) "{v[204:211]}"(a.con), [srd] "s"(a.srd), [lb] "s"(a.lb)
DEV void attn_load_q(Attn& a, unsigned q, unsigned vqm, unsigned vqr) {
    asm volatile("buffer_load_dwordx4 " R_Q0M ", %[m], %[srd], %[q] offen sc1\n\tbuffer_load_dwordx4 " R_Q1M ", %[m], %[srd], %[q] offen offset:2560 sc1\n\t"
                 "buffer_load_dwordx2 " R_Q0R ", %[r], %[srd], %[q] offen sc1\n\tbuffer_load_dwordx2 " R_Q1R ", %[r], %[srd], %[q] offen offset:2560 sc1"
                 : "=&{v[192:199]}"(a.qm), "=&{v[200:203]}"(a.qr) : [srd] "s"(a.srd), [q] "s"(q), [m] "v"(vqm), [r] "v"(vqr) : "memory");
}
DEV void attn_fetch(Attn& a) { asm volatile(FETCH : "+{s[60:63]}"(a.ss) : ATTN_DMA(a) : "memory", "scc"); }
#define ZERO8(r) "v_mov_b32 v[" #r "], 0\n\tv_mov_b32 v[" #r "+1], 0\n\tv_mov_b32 v[" #r "+2], 0\n\tv_mov_b32 v[" #r "+3], 0\n\t" \
                 "v_mov_b32 v[" #r "+4], 0\n\tv_mov_b32 v[" #r "+5], 0\n\tv_mov_b32 v[" #r "+6], 0\n\tv_mov_b32 v[" #r "+7], 0"
template <int P> DEV void attn_init_o(Attn& a) {
    if constexpr (P == 0) asm volatile(ZERO8(128) : "={v[128:159]}"(a.oo));
    if constexpr (P == 1) asm volatile(ZERO8(136) : "+{v[128:159]}"(a.oo));
    if constexpr (P == 2) asm volatile(ZERO8(144) : "+{v[128:159]}"(a.oo));
    if constexpr (P == 3) asm volatile(ZERO8(152) : "+{v[128:159]}"(a.oo));
}
#define SCORES0 QK1(0, 0, 0) QK1(1, 0, 1) QK2(0, 0, 0) QK2(1, 0, 1) "s_nop 11\n\t"
template <int N> DEV void attn_begin(Attn& a) {
    asm volatile("s_waitcnt vmcnt(%[n]) lgkmcnt(0)\n\ts_barrier\n\t" RDK(0, 0) RDK(1, 2848) RDV(0, 0) "s_waitcnt lgkmcnt(4)\n\t" SCORES0 LGK0
                 : "=&{v[64:95]}"(a.sa), "+{v[160:191]}"(a.kv), "+{v[192:199]}"(a.qm), "+{v[200:203]}"(a.qr) : [n] "i"(N) : "memory");
}
DEV void attn_scores0(Attn& a) {
    asm volatile("s_nop 1\n\t" SCORES0 : "=&{v[64:95]}"(a.sa) : "{v[160:191]}"(a.kv), "{v[192:199]}"(a.qm), "{v[200:203]}"(a.qr));
}
#define ATTN_STATE2(a) "+{v[64:95]}"(a.sa), "+{v[128:159]}"(a.oo), "+{v[160:191]}"(a.kv), "+{s[60:63]}"(a.ss), "+{v[204:211]}"(a.con)
#define ATTN_STATE(a) ATTN_STATE2(a), "+{v[96:127]}"(a.sb)
#define ATTN_IN(a) "{v[192:199]}"(a.qm), "{v[200:203]}"(a.qr), [srd] "s"(a.srd), [lb] "s"(a.lb), [dn] "s"(-8 * KVT), [up] "s"(4 * KVT)
template <int X> DEV void attn_iter_first(Attn& a) {
    asm volatile("s_movk_i32 s62, 22784\n\t" ITER_X(PERIOD_NOPV(0, 2, 1, 0), "1", "%[n]", NOFETCH, , , , )
                 : ATTN_STATE2(a), "=&{v[96:127]}"(a.sb) : ATTN_IN(a), [n] "i"(X) : "memory", "scc");
}
template <int X> DEV void attn_iter(Attn& a) {
    asm volatile(ITER_X(PE0, "1", "%[n]", FETCH, , , , ) "s_waitcnt vmcnt(3)" : ATTN_STATE(a) : ATTN_IN(a), [n] "i"(X) : "memory", "scc");
}
DEV void attn_loop(Attn& a, unsigned n) {
    asm volatile("s_cmp_eq_u32 %[n], 0\n\ts_cbranch_scc1 3f\n\t2:\n\t" ITER_X(PE0, "1", "0", FETCH, , , , )
                 "s_sub_u32 %[n], %[n], 1\n\ts_cmp_lg_u32 %[n], 0\n\ts_cbranch_scc1 2b\n\t3:"
                 : ATTN_STATE(a), [n] "+s"(n) : ATTN_IN(a) : "memory", "scc");
}
#define PF1(r, v, p, o) "buffer_load_dwordx4 v[" #r ":" #r "+3], %[" #v "], %[srd], %[" #p "] offen offset:" #o " sc1\n\t"
#define NX(p) "s_add_u32 %[" #p "], %[" #p "], 69632\n\t"
#define PF3(r0, r1, r2, p) PF1(r0, v, p, 0) PF1(r1, v, p, 1024) PF1(r2, v, p, 2048) NX(p)
#define PF5(r0, r1, r2, r3, r4) PF1(r0, v, pb, 0) PF1(r1, v, pb, 1024) PF1(r2, v, pb, 2048) PF1(r3, v, pb, 3072) PF1(r4, v2, pb, 3072) NX(pb)
struct Pre { u32x32 a, b, c; u32x4 d; };
DEV void attn_rest(Attn& a, int kh, unsigned n, Pre& p, unsigned pa, unsigned pb, unsigned voff) {
    asm volatile(
        "s_cmp_lg_u32 %[kh], 0\n\ts_cbranch_scc1 5f\n\t"
        ITER_X(PE0, "1", "0", NOFETCH, , PF3(0, 4, 8, pb) PF1(60, v, pa, 0) PF1(212, v, pa, 1024), PF3(12, 16, 20, pb) PF1(240, v, pa, 2048) NX(pa) PF1(216, v, pa, 0),
               PF3(24, 28, 32, pb) PF1(220, v, pa, 1024) PF1(244, v, pa, 2048) NX(pa))
        ITER_LAST(PF3(36, 40, 44, pb) PF1(224, v, pa, 0) PF1(228, v, pa, 1024) NX(pa), PF3(48, 52, 56, pb) PF1(232, v, pa, 0) PF1(236, v, pa, 1024), , )
        "s_branch 6f\n\t5:\n\t"
        ITER_X(PE0, "2", "0", FETCH, , PF5(0, 4, 8, 12, 16), PF5(20, 24, 28, 32, 36), PF5(40, 44, 48, 52, 56))
        ITER_X(PE0, "3", "5", FETCH, , PF5(60, 212, 216, 220, 224), PF5(228, 232, 236, 240, 244), )
        "s_cmp_eq_u32 %[n], 0\n\ts_cbranch_scc1 8f\n\t7:\n\t" ITER_X(PE0, "4", "0", FETCH, , , , )
        "s_sub_u32 %[n], %[n], 1\n\ts_cmp_lg_u32 %[n], 0\n\ts_cbranch_scc1 7b\n\t8:\n\t"
        ITER_X(PE0, "9", "0", NOFETCH, , , , ) ITER_LAST(, , , )
        "6:"
        : ATTN_STATE(a), "=&{v[0:31]}"(p.a), "=&{v[32:63]}"(p.b), "=&{v[212:243]}"(p.c), "=&{v[244:247]}"(p.d), [pa] "+s"(pa), [pb] "+s"(pb), [n] "+s"(n)
        : ATTN_IN(a), [v] "v"(voff), [v2] "v"(voff + 1024), [kh] "s"(kh)
        : "memory", "scc");
}
template <int I> DEV u32x4 pre_frag(const Pre& p) {
    if constexpr (I < 8) return (u32x4){p.a[4 * I], p.a[4 * I + 1], p.a[4 * I + 2], p.a[4 * I + 3]};
    else if constexpr (I < 16) return (u32x4){p.b[4 * I - 32], p.b[4 * I - 31], p.b[4 * I - 30], p.b[4 * I - 29]};
    else if constexpr (I < 24) return (u32x4){p.c[4 * I - 64], p.c[4 * I - 63], p.c[4 * I - 62], p.c[4 * I - 61]};
    else return p.d;
}
template <int O, class V> DEV float max16(const V& s) {
    float a = fmaxf(fmaxf(s[O], s[O + 1]), s[O + 2]), b = fmaxf(fmaxf(s[O + 3], s[O + 4]), s[O + 5]);
    a = fmaxf(fmaxf(a, s[O + 6]), s[O + 7]); b = fmaxf(fmaxf(b, s[O + 8]), s[O + 9]);
    a = fmaxf(fmaxf(a, s[O + 10]), s[O + 11]); b = fmaxf(fmaxf(b, s[O + 12]), s[O + 13]);
    a = fmaxf(fmaxf(a, s[O + 14]), s[O + 15]);
    return fmaxf(a, b);
}
DEV void attn_set_reference(Attn& a, float (&c)[2], int hh) {
#pragma unroll
    for (int i = 0; i < 2; ++i) {
        c[i] = swap_max(c[i]);
        const u32x2 t = split3(-c[i]);
        a.qr[2 * i] = hh ? t[0] : a.qr[2 * i]; a.qr[2 * i + 1] = hh ? t[1] : a.qr[2 * i + 1];
    }
}

typedef __bf16 bf16x16 __attribute__((ext_vector_type(16)));
typedef float f32x8 __attribute__((ext_vector_type(8)));
DEV void attn_check_redo(Attn& a, float (&c)[2], __amdgpu_buffer_rsrc_t wsr, unsigned kvoff, int odd, int ntiles) {
    float chk0 = (a.oo[8] * 0x1p30f) * 0.f, chk1 = (a.oo[24] * 0x1p30f) * 0.f;
#pragma unroll
    for (int i = 0; i < 12; ++i) { chk0 = __builtin_fmaf(a.oo[i], 0.f, chk0); chk1 = __builtin_fmaf(a.oo[16 + i], 0.f, chk1); }
    if (__builtin_expect(__any(!(chk0 + chk1 == 0.f)), 0)) {
        asm volatile("s_waitcnt vmcnt(0)" ::: "memory");
        const int lane = lane_id(), r32 = lane & 31, hh = lane >> 5;
        const unsigned vk = odd * 1536 + lane * 16, vr = hh ? OOB : 1024 + r32 * 16 + odd * 8, vv = r32 < 20 ? 2560 + odd * 1280 + hh * 320 + r32 * 16 : OOB;
        const unsigned one = r32 == 20 ? 0x3f803f80u : 0u;
        const u32x4 qm[2] = {{a.qm[0], a.qm[1], a.qm[2], a.qm[3]}, {a.qm[4], a.qm[5], a.qm[6], a.qm[7]}};
        const u32x2 qr[2] = {{hh ? 0u : a.qr[0], hh ? 0u : a.qr[1]}, {hh ? 0u : a.qr[2], hh ? 0u : a.qr[3]}};
        f32x16 o[2] = {};
        c[0] = -INFINITY; c[1] = -INFINITY;
#pragma unroll 1
        for (int pass = 0; pass < 2; ++pass) {
#pragma unroll 1
            for (int t = 0; t < ntiles; ++t) {
                const u32x4 km = __builtin_amdgcn_raw_buffer_load_b128(wsr, vk, kvoff + t * KVS, SC1);
                const u32x2 kr = __builtin_amdgcn_raw_buffer_load_b64(wsr, vr, kvoff + t * KVS, SC1);
                const u32x4 va = __builtin_amdgcn_raw_buffer_load_b128(wsr, vv, kvoff + t * KVS, SC1) | one;
                const u32x4 vb = __builtin_amdgcn_raw_buffer_load_b128(wsr, vv + 640, kvoff + t * KVS, SC1) | one;
#pragma unroll
                for (int i = 0; i < 2; ++i) {
                    f32x16 s = MFMA32(__builtin_bit_cast(bf16x8, km), __builtin_bit_cast(bf16x8, qm[i]), (f32x16){});
                    s = MFMA8(kr, qr[i], s);
                    if (pass == 0) c[i] = fmaxf(c[i], max16<0>(s));
                    else {
                        f32x8 pl, ph;
#pragma unroll
                        for (int e = 0; e < 8; ++e) { pl[e] = __builtin_amdgcn_exp2f(s[e] - c[i]); ph[e] = __builtin_amdgcn_exp2f(s[8 + e] - c[i]); }
                        o[i] = __builtin_amdgcn_smfmac_f32_32x32x32_bf16(__builtin_bit_cast(bf16x8, va), __builtin_bit_cast(bf16x16, pl), o[i], 0xdddd, 0, 0);
                        o[i] = __builtin_amdgcn_smfmac_f32_32x32x32_bf16(__builtin_bit_cast(bf16x8, vb), __builtin_bit_cast(bf16x16, ph), o[i], 0xdddd, 0, 0);
                    }
                }
            }
            c[0] = swap_max(c[0]); c[1] = swap_max(c[1]);
        }
#pragma unroll
        for (int e = 0; e < 16; ++e) { a.oo[e] = o[0][e]; a.oo[16 + e] = o[1][e]; }
    }
}

struct Ring {
    const char *x, *wq, *wgu;
    unsigned xo0, xo1, wo0, wo1, go0, go1, go2;
    unsigned lds0; int w, s0;
};
template <int C, int P> DEV void ring_piece(const Ring& r) {
    if constexpr (C < 20) {
        int cc = r.s0 + C % 10; cc = cc >= 10 ? cc - 10 : cc;
        if constexpr (C < 10) {
            const unsigned d = r.lds0 + (C & 3) * 32768 + r.w * 2048;
            if constexpr (P == 0) glds16(r.xo0, r.x + cc * 128, d);
            if constexpr (P == 1) glds16(r.xo1, r.x + cc * 128, d + 1024);
            if constexpr (P == 2) glds16(r.wo0, r.wq + cc * (64 * 3840), d + 16384);
            if constexpr (P == 3) glds16(r.wo1, r.wq + cc * (64 * 3840), d + 16384 + 1024);
        } else {
            const unsigned d = r.lds0 + (C & 3) * 32768 + r.w * 3072 + (r.w >> 2) * 32;
            const char* b = r.wgu + cc * (64 * 3072);
            if constexpr (P == 0) glds16(r.go0, b, d);
            if constexpr (P == 1) glds16(r.go1, b, d + 1024);
            if constexpr (P == 2) glds16(r.go2, b, d + 2048);
        }
    }
}
template <int C> DEV void ring_issue(const Ring& r) { ring_piece<C, 0>(r); ring_piece<C, 1>(r); ring_piece<C, 2>(r); ring_piece<C, 3>(r); }
constexpr int ring_n(int c) { return c < 10 ? 4 : (c < 20 ? 3 : 0); }
constexpr int vm_pre(int c) { return c == 6 ? 4 : (c == 15 ? 5 : 0); }
constexpr int vm_post(int c) { return ((c == 2 || c == 11) ? 1 : 0) + (c == 17 ? 7 : 0); }
constexpr int vm_wait(int c) {
    if (c == 9) return 63;
    if (c == 10) return 4 + 6;
    if (c == 11) return 6;
    return vm_post(c - 3) + vm_pre(c - 2) + ring_n(c + 2) + vm_post(c - 2) + vm_pre(c - 1) + ring_n(c + 3) + vm_post(c - 1);
}

__global__ __launch_bounds__(512, 2) void fused_block(const char* x, const char* wqkv, const char* wo, const char* wg, const char* wu, const char* wd,
                                                      char* out, char* ws) {
    __shared__ __attribute__((aligned(16))) char smem[L_TOTAL];
    LAS char* lds = (LAS char*)smem;
    const int w = __builtin_amdgcn_readfirstlane(threadIdx.x >> 6);
    const int bid = blockIdx.x, xcd = bid & 7, rr = bid >> 3;
#define LANE_IDS const int lane = lane_id(); \
    const int tid = 64 * w + lane, r32 = lane & 31, hh = lane >> 5; (void)tid; (void)r32; (void)hh
    asm volatile("" : "+s"(x), "+s"(wqkv), "+s"(wo), "+s"(wg), "+s"(wu), "+s"(wd), "+s"(out), "+s"(ws));
    const __amdgpu_buffer_rsrc_t wsr = __builtin_amdgcn_make_buffer_rsrc(ws, 0, WS_END, 0x00020000);
    LAS unsigned* lflag = (LAS unsigned*)(lds + L_FLAG);
    u64 tag;
    STAMP_DECL;
    STAMP(0);
    const int mi = rr & 7, ji = rr >> 3, m = 8 * (xcd & 1) + mi, j = 4 * (xcd >> 1) + ji;
    const int wm = w & 3, wn = w >> 2;
    const int g = 4 * xcd + (rr >> 3), mq = rr & 7, qq = w & 3, kh = w >> 2, odd = g & 1;
    const unsigned qoff = WS_Q + ((g >> 1) * 64 + 8 * mq + 2 * qq) * 2560;
    const unsigned kvoff = WS_KV + ((g >> 1) * 64 + NT0 * kh) * KVS;
    Attn at;
    at.srd = (u32x4){(unsigned)(size_t)ws, (unsigned)((size_t)ws >> 32) & 0xffffu, WS_END, 0x00020000u};
    float c[2];
    {
        LANE_IDS;
        Ring rg;
        rg.x = x + m * 128 * 1280; rg.wq = wqkv + (j < 15 ? j : 14) * 256; rg.wgu = (w < 4 ? wg : wu) + j * 192;
        rg.lds0 = (unsigned)(size_t)lds; rg.w = w;
        rg.s0 = (((5 * ji + 1) >> 1) + mi + (mi >> 2)) % 10;
        rg.xo0 = (16 * w + (lane >> 3)) * 1280 + (((lane & 7) ^ (lane >> 4)) << 4);
        rg.xo1 = (16 * w + 8 + (lane >> 3)) * 1280 + (((lane & 7) ^ (4 + (lane >> 4))) << 4);
        rg.wo0 = (8 * w + (lane >> 4)) * 3840 + (((lane & 15) ^ ((lane >> 4) << 2)) << 4);
        rg.wo1 = rg.wo0 + 4 * 3840;
        if (w == 7) {
            const char* pc; asm volatile("s_getpc_b64 %0" : "=s"(pc));
            glds4((rr * CODE_LINES + min(lane, CODE_LINES - 1)) * 128, pc, rg.lds0 + L_TR);
        }
        ring_issue<0>(rg); __builtin_amdgcn_s_barrier();
        ring_issue<1>(rg);
        asm volatile("s_load_dwordx2 %0, %1, 0x0\n\ts_waitcnt lgkmcnt(0)" : "=s"(tag) : "s"(ws) : "memory");
        tag += 1;
        const unsigned tag32 = (unsigned)tag;
        { const int p0 = (w & 3) * 192 + lane, p1 = p0 + 64, p2 = p0 + 128;
          rg.go0 = (p0 / 12) * 3072 + (p0 % 12) * 16; rg.go1 = (p1 / 12) * 3072 + (p1 % 12) * 16; rg.go2 = (p2 / 12) * 3072 + (p2 % 12) * 16; }
        if (tid < 8) lflag[tid] = 0u;

        const int g4 = lane >> 4, q4 = (lane >> 2) & 3, p4 = lane & 3, rot = wm ^ (2 * wn);
        const int xrow = 32 * wm + r32;
        const unsigned xa = (xrow * 128 + ((hh ^ ((xrow >> 1) & 7)) << 4)) ^ (rot << 5);
        const unsigned wab = 16384 + (rot << 12) + (8 * (g4 >> 1) + q4) * 256 + (g4 & 1) * 32 + (p4 & 1) * 16 + (p4 >> 1) * 8;
        const unsigned wa0 = wab + (((2 * wn) ^ q4) << 6), wa1 = wab + (((2 * wn + 1) ^ q4) << 6);
        LAS float* part = (LAS float*)(lds + L_PART); LAS u32x2* fin = (LAS u32x2*)(lds + L_FIN);
        bf16x8 xf[40], fw[2][3];
        f32x16 acc[2] = {};
        float rs = 0.f, rq = 0.f, cs0 = 0.f, cs1 = 0.f, pv[4];
        u32x2 gr[4], fa;
        const unsigned col2 = (mi & 3) == 3 ? 0x3f803f80u : 0u;
        int pw_row = (2 * wn + hh) * 384 + xrow, pw_col = wm * 384 + 256 + 64 * wn + lane, pr_i = w < 6 ? tid : lane;
        unsigned pub_o = tid * 8 + (w < 6 ? WS_GA + bid * 3072 : OOB);
        asm volatile("" : "+v"(pw_row), "+v"(pw_col), "+v"(pr_i), "+v"(pub_o), "+v"(rg.go0), "+v"(rg.go1), "+v"(rg.go2));
        const unsigned gab = w < 4 ? WS_GA + (xcd + 8 * mi) * 3072 + w * 512 : WS_GA + (xcd + 64 * ji + 32 * ((w >> 1) & 1)) * 3072 + 2048 + (w & 1) * 512;
        const unsigned gao = gab + lane * 8, gas = w < 4 ? 64 * 3072 : 8 * 3072;
        unsigned wadr[2][4], xadr[4];
#pragma unroll
        for (int k = 0; k < 4; ++k) { wadr[0][k] = wa0 ^ (k << 12); wadr[1][k] = wa1 ^ (k << 12); xadr[k] = xa ^ (k << 5); }
#define FLIP(c, a) do { if constexpr ((c) > 0 && ((c) & 1) == 0) a ^= 0x10000u; if constexpr (((c) & 1) == 0) asm volatile("" : "+v"(a)); } while (0)
#define RW1(f, b, c, k) FLIP(c, wadr[b][k]); fw[f][b] = ldstr(lds + ((c) & 1) * 32768 + wadr[b][k], lds + ((c) & 1) * 32768 + wadr[b][k] + 1024)
#define RX1(c, k) FLIP(c, xadr[k]); xf[4 * (c) + (k)] = lds128(lds + ((c) & 1) * 32768 + xadr[k])
#define M1(f, b, c, k) acc[b] = MFMA32(fw[f][b], xf[4 * (c) + (k)], acc[b]); SBAR()
        STAMPP(2, 1);
        wait_bar<4>();
        STAMPP(2, 2);
        RW1(0, 0, 0, 0); RX1(0, 0); RW1(0, 1, 0, 0); RW1(1, 0, 0, 1); RX1(0, 1); RW1(1, 1, 0, 1); SBAR();
        ring_issue<2>(rg); ring_piece<3, 0>(rg); SBAR();
#define A1C(c, H3, H4, H5, H6) do { constexpr int n_ = c < 9 ? c + 1 : 0; \
        M1(0, 0, c, 0); RW1(0, 0, c, 2); RX1(c, 2); if constexpr (c < 9) ring_piece<c + 3, 1>(rg); SBAR(); \
        M1(0, 1, c, 0); RW1(0, 1, c, 2); if constexpr (c < 9) ring_piece<c + 3, 2>(rg); SBAR(); \
        M1(1, 0, c, 1); RW1(1, 0, c, 3); RX1(c, 3); ring_piece<c + 3, 3>(rg); SBAR(); \
        M1(1, 1, c, 1); RW1(1, 1, c, 3); H3; SBAR(); \
        M1(0, 0, c, 2); H4; SBAR(); M1(0, 1, c, 2); H5; SBAR(); \
        wait_bar<vm_wait(c)>(); \
        M1(1, 0, c, 3); \
        if constexpr (c < 9) { RW1(0, 0, n_, 0); RX1(n_, 0); RW1(0, 1, n_, 0); RW1(1, 0, n_, 1); RX1(n_, 1); } \
        H6; SBAR(); \
        M1(1, 1, c, 3); \
        if constexpr (c < 9) { RW1(1, 1, n_, 1); } \
        if constexpr (c < 8) ring_piece<c + 4, 0>(rg); \
        SBAR(); } while (0)
        A1C(0, , , ({ rs = dot_ones(xf[0], rs); rq = dot_self(xf[0], rq); rs = dot_ones(xf[1], rs); rq = dot_self(xf[1], rq);
                      cs0 = dot_ones(fw[0][0], cs0); cs1 = dot_ones(fw[0][1], cs1); }), );
        STAMPP(2, 3);
        A1C(1, , , ({ rs = dot_ones(xf[4], rs); rq = dot_self(xf[4], rq); rs = dot_ones(xf[5], rs); rq = dot_self(xf[5], rq);
                      cs0 = dot_ones(fw[0][0], cs0, col2); cs1 = dot_ones(fw[0][1], cs1, col2); }), );
        STAMPP(2, 4);
        A1C(2, , ({ cs0 = swap_add(cs0); cs1 = swap_add(cs1); part[pw_col] = hh ? cs1 : cs0; }),
            ({ float r2 = dot_ones(xf[8], rs), q2 = dot_self(xf[8], rq); r2 = dot_ones(xf[9], r2); q2 = dot_self(xf[9], q2);
               part[pw_row] = (ji & 1) ? rs : r2; part[pw_row + 128] = (ji & 1) ? rq : q2; }),
            ({ pv[0] = part[pr_i]; pv[1] = part[384 + pr_i]; pv[2] = part[768 + pr_i]; pv[3] = part[1152 + pr_i]; }));
        STAMPP(2, 5);
        A1C(3, , __builtin_amdgcn_raw_buffer_store_b64((u32x2){__float_as_uint((pv[0] + pv[1]) + (pv[2] + pv[3])), tag32}, wsr, pub_o, 0, SC1), , );
        STAMPP(2, 6); A1C(4, , , , ); STAMPP(2, 7); A1C(5, , , , ); STAMPP(2, 8);
        A1C(6, , , , ({ gr[0] = ld2_async(ws, gao); gr[1] = ld2_async(ws, gao + gas); gr[2] = ld2_async(ws, gao + 2 * gas); gr[3] = ld2_async(ws, gao + 3 * gas); }));
        STAMPP(2, 9); A1C(7, , , , ); STAMPP(2, 10);
        float rstd, st0, st1;
        unsigned nmu; u32x2 cf[3];
        A1C(8, ({ asm volatile("s_waitcnt vmcnt(6)" : "+v"(gr[0]), "+v"(gr[1]), "+v"(gr[2]), "+v"(gr[3]) :: "memory");
                  st0 = gran_sum(ws, gao, gas, gr, tag32); }),
            ({ u32x2 t = split3(st0); if (w < 4) t[0] = __float_as_uint(st0);
               fin[(w < 6 ? 64 * w : 64 * w + 64) + lane] = t; }), ,
            ({ st0 = *(const LAS float*)(fin + xrow); st1 = *(const LAS float*)(fin + 128 + xrow);
               cf[0] = fin[256 + 192 * hh + 64 * wn + r32]; cf[1] = fin[256 + 192 * hh + 64 * wn + 32 + r32]; }));
        A1C(9, ({ const float mean = st0 * (1.f / 640.f), var = fmaxf(st1 * (1.f / 640.f) - mean * mean, 0.f);
                  nmu = pk2(-mean, -mean);
                  rstd = __builtin_amdgcn_rcpf(bfr(__builtin_amdgcn_sqrtf(bfr(bfr(var) + 1e-5f))));
                  asm volatile("" : "+v"(nmu), "+v"(rstd)); }),
            , ({ acc[0] = MFMA8(cf[0], ((u32x2){nmu, nmu}), acc[0]); acc[1] = MFMA8(cf[1], ((u32x2){nmu, nmu}), acc[1]); }), );
        asm volatile("" : "+{v[64:67]}"(xf[28]), "+{v[68:71]}"(xf[29]), "+{v[72:75]}"(xf[30]), "+{v[76:79]}"(xf[31]), "+{v[80:83]}"(xf[32]), "+{v[84:87]}"(xf[33]),
                          "+{v[88:91]}"(xf[34]), "+{v[92:95]}"(xf[35]), "+{v[96:99]}"(xf[36]), "+{v[100:103]}"(xf[37]), "+{v[104:107]}"(xf[38]), "+{v[108:111]}"(xf[39]));
        STAMPP(2, 11);
        STAMPP(1, 1);
        { const int tile = 4 * m + wm;
          const float sc = j < 5 ? rstd * QSCALE : rstd;
          u32x4 y[4];
#pragma unroll
          for (int i = 0; i < 4; ++i)
#pragma unroll
              for (int e = 0; e < 4; ++e) y[i][e] = pk2(sc * acc[i >> 1][8 * (i & 1) + 2 * e], sc * acc[i >> 1][8 * (i & 1) + 2 * e + 1]);
          if (j < 10) {
              const unsigned base = (j < 5 ? WS_Q + tile * 2560 : WS_KV + tile * KVS) + r32 * 16, ps = j < 5 ? 64 * 2560 : 64 * KVS;
              const int c8 = (j < 5 ? j : j - 5) * 16 + 8 * wn + hh;
#pragma unroll
              for (int i = 0; i < 4; ++i) {
                  const int ci = c8 + 2 * i, pr = ci / 5, pl = ci - 5 * pr;
                  __builtin_amdgcn_raw_buffer_store_b128(y[i], wsr, base + pr * ps + pl * 512, 0, SC1);
              }
          } else {
              LAS char* tp = lds + L_TR + w * 2048;
              const int cu = 2 * (g4 & 1) + (p4 >> 1);
#pragma unroll
              for (int b = 0; b < 2; ++b) {
#pragma unroll
                  for (int v = 0; v < 2; ++v) *(LAS u32x4*)(tp + r32 * 64 + (((2 * v + hh) ^ ((r32 >> 1) & 3)) << 4)) = y[2 * b + v];
                  asm volatile("s_waitcnt lgkmcnt(0)" ::: "memory");
                  const int col = (j - 10) * 128 + 64 * wn + 32 * b + r32, head = col / 20, d = col - 20 * head;
                  const unsigned base = j < 15 ? WS_KV + ((head >> 1) * 64 + tile) * KVS + 2560 + (head & 1) * 1280 + d * 16 + hh * 320 : OOB;
#pragma unroll
                  for (int i = 0; i < 2; ++i) {
                      const int r0 = 16 * i + 8 * hh + q4;
                      const bf16x8 v = ldstr(tp + r0 * 64 + ((cu ^ (q4 >> 1)) << 4) + (p4 & 1) * 8,
                                             tp + (r0 + 4) * 64 + ((cu ^ (2 + (q4 >> 1))) << 4) + (p4 & 1) * 8);
                      __builtin_amdgcn_raw_buffer_store_b128(__builtin_bit_cast(u32x4, v), wsr, base + i * 640, 0, SC1);
                  }
                  asm volatile("s_waitcnt lgkmcnt(0)" ::: "memory");
              }
          } }
        STAMPP(1, 2); STAMPP(4, 1);
        unsigned ga[4];
#pragma unroll
        for (int ks = 0; ks < 4; ++ks) ga[ks] = (g4 & 1) * (12288 + 32) + (8 * (g4 >> 1) + q4) * 192 + wn * 96 + p4 * 8 + (ks ^ rot) * 3072;
        float cs[3] = {0.f, 0.f, 0.f};
        f32x16 ac2[3] = {};
#define RW2(f, b, c, k) do { if constexpr (b == 0) FLIP(c, ga[k]); fw[f][b] = ldstr(lds + ((c) & 1) * 32768 + ga[k] + b * 32, lds + ((c) & 1) * 32768 + ga[k] + b * 32 + 768); } while (0)
#define M2(f, b, c, k) ac2[b] = MFMA32(fw[f][b], xf[4 * ((c) - 10) + (k)], ac2[b]); SBAR()
        SBAR(); wait_bar<3 + 4>();
        RW2(0, 0, 10, 0); RW2(0, 1, 10, 0); RW2(0, 2, 10, 0); RW2(1, 0, 10, 1); RW2(1, 1, 10, 1); RW2(1, 2, 10, 1); SBAR();
#define A2C(c, H0, H1, H2, H3, H5, H6) do { \
        M2(0, 0, c, 0); RW2(0, 0, c, 2); H0; SBAR(); \
        M2(0, 1, c, 0); RW2(0, 1, c, 2); H1; SBAR(); \
        M2(0, 2, c, 0); RW2(0, 2, c, 2); H2; SBAR(); \
        M2(1, 0, c, 1); RW2(1, 0, c, 3); H3; SBAR(); \
        M2(1, 1, c, 1); RW2(1, 1, c, 3); SBAR(); \
        M2(1, 2, c, 1); RW2(1, 2, c, 3); SBAR(); \
        M2(0, 0, c, 2); M2(0, 1, c, 2); H5; SBAR(); \
        if constexpr (c < 19) wait_bar<vm_wait(c)>(); \
        M2(0, 2, c, 2); \
        if constexpr (c < 19) { RW2(0, 0, c + 1, 0); RW2(0, 1, c + 1, 0); RW2(0, 2, c + 1, 0); } \
        H6; SBAR(); \
        M2(1, 0, c, 3); if constexpr (c < 19) RW2(1, 0, c + 1, 1); ring_piece<c + 4, 0>(rg); SBAR(); \
        M2(1, 1, c, 3); if constexpr (c < 19) RW2(1, 1, c + 1, 1); ring_piece<c + 4, 1>(rg); SBAR(); \
        M2(1, 2, c, 3); if constexpr (c < 19) RW2(1, 2, c + 1, 1); ring_piece<c + 4, 2>(rg); SBAR(); STAMPP(4, c - 8); } while (0)
        A2C(10, ring_issue<12>(rg), , ring_issue<13>(rg), , ({ cs[0] = dot_ones(fw[0][0], cs[0]); cs[1] = dot_ones(fw[0][1], cs[1]); cs[2] = dot_ones(fw[0][2], cs[2]); }), );
        A2C(11, , , , , ({ cs[0] = dot_ones(fw[0][0], cs[0], col2); cs[1] = dot_ones(fw[0][1], cs[1], col2); cs[2] = dot_ones(fw[0][2], cs[2], col2);
                           cs[0] = swap_add(cs[0]); cs[1] = swap_add(cs[1]); cs[2] = swap_add(cs[2]);
                           LANE_IDS; part[wm * 384 + 96 * wn + lane] = hh ? cs[1] : cs[0]; part[wm * 384 + 96 * wn + 64 + r32] = cs[2]; }),
            ({ const int pi = (w < 3 ? 64 * w : 0) + lane_id(); pv[0] = part[pi]; pv[1] = part[384 + pi]; pv[2] = part[768 + pi]; pv[3] = part[1152 + pi]; }));
        if (w == 0 && lane_id() == 0) flag_store(ws + WS_FA + bid * 8, tag);
        A2C(12, __builtin_amdgcn_raw_buffer_store_b64((u32x2){__float_as_uint((pv[0] + pv[1]) + (pv[2] + pv[3])), tag32}, wsr, lane_id() * 8 + (w < 3 ? WS_GB + bid * 1536 + w * 512 : OOB), 0, SC1), , , , , );
        A2C(13, , , , , , );
        A2C(14, , , , , , );
        { LANE_IDS;
        const int gbh = w >= 3 && w < 6;
        const unsigned gbo = WS_GB + (xcd + 64 * ji + 32 * gbh) * 1536 + (w < 6 ? w - 3 * gbh : 0) * 512 + lane * 8, fao = WS_FA + (tid & 255) * 8;
        A2C(15, , , , , , ({ gr[0] = ld2_async(ws, gbo); gr[1] = ld2_async(ws, gbo + 8 * 1536); gr[2] = ld2_async(ws, gbo + 16 * 1536); gr[3] = ld2_async(ws, gbo + 24 * 1536);
                   fa = ld2_async(ws, fao); }));
        A2C(16, , , , , , );
        A2C(17, ({ asm volatile("s_waitcnt vmcnt(3)" : "+v"(gr[0]), "+v"(gr[1]), "+v"(gr[2]), "+v"(gr[3]), "+v"(fa) :: "memory");
                   st0 = gran_sum(ws, gbo, 8 * 1536, gr, tag32); }),
            flag_wait(ws, fao, fa, tag),
            fin[256 + (w < 6 ? tid : lane)] = split3(st0),
            ({ const unsigned ring = (unsigned)(size_t)lds + (kh ? L_KV1 : L_KV0), vs = 2560u + odd * 1280 + lane * 16;
               at.lb = ring + qq * KVT;
               at.ss = (u32x4){kvoff + qq * KVS, 0u, 0u, 0u};
               at.con = (u32x8){0xddddu, odd * 1024u + lane * 16, hh ? vs - 1536 : odd * 1024u + lane * 16, vs - 1536, (unsigned)(size_t)lds + L_FLAG + 12 * kh, 1u, 0u, 0u};
               asm volatile("" : "+{v[204:211]}"(at.con)); }), ,
            ({ cf[0] = fin[256 + 192 * hh + 96 * wn + r32]; cf[1] = fin[256 + 192 * hh + 96 * wn + 32 + r32]; cf[2] = fin[256 + 192 * hh + 96 * wn + 64 + r32]; }));
        A2C(18, attn_fetch(at), , attn_load_q(at, qoff, odd * 1536 + lane * 16, hh ? OOB : 1024 + r32 * 16 + odd * 8),
            ({ ac2[0] = MFMA8(cf[0], ((u32x2){nmu, nmu}), ac2[0]); ac2[1] = MFMA8(cf[1], ((u32x2){nmu, nmu}), ac2[1]); ac2[2] = MFMA8(cf[2], ((u32x2){nmu, nmu}), ac2[2]); }), , attn_init_o<0>(at));
        A2C(19, attn_fetch(at), attn_init_o<1>(at),
            ({
               const unsigned ring = (unsigned)(size_t)lds + (kh ? L_KV1 : L_KV0), va = ring + 1536 + hh * 320 + r32 * 16, vx = ring + (r32 == 20 ? 2816 : 2832);
               at.kv[28] = ring + odd * 512 + lane * 16; at.kv[29] = ring + (hh ? 2816 : (odd ? 8 : 1024) + r32 * 16);
               at.kv[30] = r32 < 20 ? va : vx; at.kv[31] = r32 < 20 ? va + 640 : vx;
               asm volatile("" : "+{v[160:191]}"(at.kv)); }),
            attn_init_o<2>(at), ,
            ({ attn_init_o<3>(at); attn_fetch(at);
               if (lane < 24) *(LAS unsigned*)(lds + (kh ? L_KV1 : L_KV0) + ((lane >> 3) * 4 + qq) * KVT + 2816 + (lane & 7) * 4) = (lane & 4) ? 0u : 0x3f803f80u; }));
        STAMPP(1, 3);
        __builtin_amdgcn_s_setprio(3);
        attn_begin<6>(at);
        { const unsigned base = WS_HP + (8 * m + 2 * wm + (r32 >> 4)) * (272 * 256) + (2 * (6 * j + 3 * wn) + hh) * 256 + (r32 & 15) * 16;
          const float r2 = rstd * rstd;
#pragma unroll
          for (int b = 0; b < 3; ++b) {
              u32x4 v;
#pragma unroll
              for (int e = 0; e < 4; ++e) v[e] = pk2(ac2[b][2 * e] * r2 * ac2[b][8 + 2 * e], ac2[b][2 * e + 1] * r2 * ac2[b][9 + 2 * e]);
              __builtin_amdgcn_raw_buffer_store_b128(v, wsr, base + b * 512, 0, SC1);
          } }
        STAMPP(1, 4); STAMPP(3, 1);
        c[0] = max16<0>(at.sa); c[1] = max16<16>(at.sa);
        attn_set_reference(at, c, hh);
        asm volatile("" : "+v"(c[0]), "+v"(c[1]));
        attn_scores0(at);
        }
    }
    Pre pre;
    const int rb = 4 * xcd + (rr >> 3), ct = rr & 7;
    const unsigned aob = WS_HP + 4 * rb * (272 * 256), bob = WS_WP + 5 * ct * (68 * 1024);
    const int krot = 3 * ((ct + (rr >> 3)) & 3);
    {
        LANE_IDS;
        const int unit = bid + 256 * w, us = unit / 10, nb = unit - 10 * us;
        const char* psrc = us < 48 ? wd : wo;
        const u32x4 psrd = {(unsigned)(size_t)psrc, (unsigned)((size_t)psrc >> 32) & 0xffffu, unit < 680 ? (us < 48 ? 1536u * 1280u : 640u * 1280u) : 0u, 0x00020000u};
        const unsigned plds = L_TR + min(w, 3) * 4096, pvo = (hh * (us < 64 ? 8 : 20) + ((lane >> 3) & 3)) * 1280 + (lane & 7) * 16;
        { const int ra = us < 48 ? 32 * us : (us < 64 ? 40 * (us - 48) : 160 * (us - 64) + 16), r1 = us < 64 ? 4 : 40, r2 = us < 48 ? 16 : (us < 64 ? 20 : 80);
#pragma unroll
          for (int i = 0; i < 4; ++i)
              asm volatile("s_mov_b32 m0, %3\n\ts_nop 0\n\tbuffer_load_dwordx4 %0, %1, %2 offen lds"
                           :: "v"(pvo), "s"(psrd), "s"((ra + (i & 1) * r1 + (i >> 1) * r2) * 1280 + nb * 128), "s"((unsigned)(size_t)lds + plds + i * 1024) : "memory"); }
        STAMPP(1, 5); STAMPP(3, 2);
        attn_iter_first<10>(at); STAMPP(3, 3);
        attn_iter<7>(at); STAMPP(3, 4);
        if (kh) arrive_publish(lflag + 1, 7u, lane, ws + WS_FH + bid * 8, tag);
        asm volatile("s_setprio 2\n\ts_cmp_eq_u32 %0, 0\n\ts_cbranch_scc1 1f\n\ts_setprio 1\n\t1:" :: "s"(kh) : "scc");
        { const unsigned pdst = lane * 16 + (unit < 680 ? WS_WP + (4 * nb * 68 + us) * 1024 : OOB);
          const LAS char* prd = lds + plds + (8 * (lane >> 4) + ((lane >> 2) & 3)) * 128 + (lane & 3) * 8;
#pragma unroll
          for (int i = 0; i < 4; ++i)
              __builtin_amdgcn_raw_buffer_store_b128(__builtin_bit_cast(u32x4, ldstr(prd + 32 * i, prd + 512 + 32 * i)), wsr, pdst + i * (68 * 1024), 0, SC1); }
        attn_iter<4>(at);
        if (!kh) arrive_publish(lflag + 1, 7u, lane, ws + WS_FH + bid * 8, tag);
        attn_loop(at, (1 - kh) * (NT0 / 4 - 6)); STAMPP(3, 5);
        u32x4 fh0 = ld4_async(ws, WS_FH + lane * 16), fh1 = ld4_async(ws, WS_FH + 1024 + lane * 16);
        attn_iter<2>(at); STAMPP(3, 6);
        asm volatile("" : "+v"(fh0), "+v"(fh1));
        flag_wait4(ws, WS_FH + lane * 16, fh0, fh1, tag);
        if (bid == 0 && tid == 0) flag_store(ws + WS_EPOCH, tag);
        STAMPP(3, 7);
        attn_rest(at, kh, (64 - NT0) / 4 - 8, pre, aob + (12 * qq + krot) * 1024, bob + (kh ? 48 + 5 * qq : 12 * qq + krot) * 1024, lane * 16);
        attn_check_redo(at, c, wsr, kvoff, odd, kh ? 64 - NT0 : NT0);
        STAMPP(1, 6); STAMPP(3, 8);
    }
    LANE_IDS;
    f32x4 acc[4][5];
    auto loada = [&](int s, u32x4 (&a)[4]) {
#pragma unroll
        for (int i = 0; i < 4; ++i) a[i] = __builtin_amdgcn_raw_buffer_load_b128(wsr, lane * 16, aob + s * 1024 + i * (272 * 256), SC1);
    };
    auto loadb = [&](int s, u32x4 (&b)[5]) {
#pragma unroll
        for (int i = 0; i < 5; ++i) b[i] = __builtin_amdgcn_raw_buffer_load_b128(wsr, lane * 16, bob + s * 1024 + i * (68 * 1024), SC1);
    };
    auto mma = [&](const u32x4 (&a)[4], const u32x4 (&b)[5]) {
#pragma unroll
        for (int i = 0; i < 4; ++i)
#pragma unroll
            for (int n = 0; n < 5; ++n) acc[i][n] = MFMA16(__builtin_bit_cast(bf16x8, b[n]), __builtin_bit_cast(bf16x8, a[i]), acc[i][n]);
    };
    if (kh == 0) {
        __builtin_amdgcn_s_setprio(0);
        { LAS char* snd = lds + L_MRG + w * 6656;
#pragma unroll
          for (int i = 0; i < 6; ++i) { const int o = 16 * (i / 3) + 4 * (i % 3); *(LAS f32x4*)(snd + i * 1024 + lane * 16) = (f32x4){at.oo[o], at.oo[o + 1], at.oo[o + 2], at.oo[o + 3]}; }
          *(LAS u32x2*)(snd + 6144 + lane * 8) = (u32x2){__float_as_uint(c[0]), __float_as_uint(c[1])};
          asm volatile("s_waitcnt lgkmcnt(0)" ::: "memory");
          if (lane == 0) __hip_atomic_store(lflag + 4 + w, 1u, __ATOMIC_RELAXED, __HIP_MEMORY_SCOPE_WORKGROUP); }
    } else {
        lds_wait(lflag + w);
        { const LAS char* rcv = lds + L_MRG + qq * 6656;
          const u32x2 cp = *(const LAS u32x2*)(rcv + 6144 + lane * 8);
          f32x4 r[6];
#pragma unroll
          for (int i = 0; i < 6; ++i) r[i] = *(const LAS f32x4*)(rcv + i * 1024 + lane * 16);
          const unsigned base = WS_HP + (16 * mq + 4 * qq + (r32 >> 4)) * (272 * 256) + (r32 & 15) * 16;
          const unsigned om = base + (192 + 2 * g + hh) * 256, orr = hh ? OOB : base + (256 + (g >> 1)) * 256 + (g & 1) * 8;
#pragma unroll
          for (int t = 0; t < 2; ++t) {
              const float co = __uint_as_float(cp[t]), cx = fmaxf(c[t], co);
              float wa = __builtin_amdgcn_exp2f(c[t] - cx), wb = __builtin_amdgcn_exp2f(co - cx);
              const float inv = __builtin_amdgcn_rcpf(swap_add(hh ? at.oo[16 * t + 8] * wa + r[3 * t + 2][0] * wb : 0.f));
              wa *= inv; wb *= inv;
              float f[12];
#pragma unroll
              for (int i = 0; i < 12; ++i) f[i] = at.oo[16 * t + i] * wa + r[3 * t + (i >> 2)][i & 3] * wb;
              __builtin_amdgcn_raw_buffer_store_b128((u32x4){pk2(f[0], f[1]), pk2(f[2], f[3]), pk2(f[4], f[5]), pk2(f[6], f[7])}, wsr, om + t * (2 * 272 * 256), 0, SC1);
              __builtin_amdgcn_raw_buffer_store_b64((u32x2){pk2(f[8], f[9]), pk2(f[10], f[11])}, wsr, orr + t * (2 * 272 * 256), 0, SC1);
          } }
        asm volatile("s_waitcnt vmcnt(0)" ::: "memory");
        arrive_publish(lflag + 2, 3u, lane, ws + WS_FO + (mq * 32 + g) * 128, tag);
    }
    STAMPP(1, 7); STAMPP(3, 9);
    asm volatile("s_waitcnt vmcnt(0)" : "+{v[0:31]}"(pre.a), "+{v[32:63]}"(pre.b), "+{v[212:243]}"(pre.c), "+{v[244:247]}"(pre.d) :: "memory");
    if (kh == 0) {
#pragma unroll
        for (int i = 0; i < 4; ++i)
#pragma unroll
            for (int n = 0; n < 5; ++n) acc[i][n] = (f32x4){};
        auto kst = [&](int i) { const int k = krot + i; return 12 * w + (k >= 12 ? k - 12 : k); };
        u32x4 a0[4] = {pre_frag<15>(pre), pre_frag<17>(pre), pre_frag<19>(pre), pre_frag<21>(pre)}, a1[4] = {pre_frag<16>(pre), pre_frag<18>(pre), pre_frag<20>(pre), pre_frag<22>(pre)};
        u32x4 a2[4] = {pre_frag<23>(pre), pre_frag<24>(pre)}, a3[4], b3[5];
        u32x4 b0[5] = {pre_frag<0>(pre), pre_frag<3>(pre), pre_frag<6>(pre), pre_frag<9>(pre), pre_frag<12>(pre)};
        u32x4 b1[5] = {pre_frag<1>(pre), pre_frag<4>(pre), pre_frag<7>(pre), pre_frag<10>(pre), pre_frag<13>(pre)};
        u32x4 b2[5] = {pre_frag<2>(pre), pre_frag<5>(pre), pre_frag<8>(pre), pre_frag<11>(pre), pre_frag<14>(pre)};
        a2[2] = __builtin_amdgcn_raw_buffer_load_b128(wsr, lane * 16, aob + kst(2) * 1024 + 2 * (272 * 256), SC1);
        a2[3] = __builtin_amdgcn_raw_buffer_load_b128(wsr, lane * 16, aob + kst(2) * 1024 + 3 * (272 * 256), SC1);
        loada(kst(3), a3); loadb(kst(3), b3); SBAR();
#pragma unroll
        for (int i = 4; i < 12; i += 4) {
            mma(a0, b0); SBAR(); loada(kst(i), a0); loadb(kst(i), b0); SBAR();
            mma(a1, b1); SBAR(); loada(kst(i + 1), a1); loadb(kst(i + 1), b1); SBAR();
            mma(a2, b2); SBAR(); loada(kst(i + 2), a2); loadb(kst(i + 2), b2); SBAR();
            mma(a3, b3); SBAR(); loada(kst(i + 3), a3); loadb(kst(i + 3), b3); SBAR();
        }
        mma(a0, b0); SBAR(); mma(a1, b1); SBAR(); mma(a2, b2); SBAR(); mma(a3, b3); SBAR();
        STAMPP(1, 8); STAMPP(3, 10);
#pragma unroll 1
        for (unsigned spin = 0; spin < SPIN_CAP; ++spin) {
            if (__all(__hip_atomic_load(lflag + 4 + (lane & 3), __ATOMIC_RELAXED, __HIP_MEMORY_SCOPE_WORKGROUP) != 0u)) break;
            __builtin_amdgcn_s_sleep(1);
        }
        asm volatile("" ::: "memory");
        LAS f32x4* red = (LAS f32x4*)(lds + L_KV0 + w * 20480);
#pragma unroll
        for (int i = 0; i < 4; ++i)
#pragma unroll
            for (int n = 0; n < 5; ++n) red[(i * 5 + n) * 64 + lane] = acc[i][n];
        STAMPP(1, 9);
        lds_bar();
        STAMPP(1, 10);
        lds_bar();
    } else {
        STAMPP(1, 8); STAMPP(3, 10);
        const int xoff = (64 * rb + 16 * qq + (lane & 15)) * 1280 + (80 * ct + 4 * (lane >> 4)) * 2;
        u32x2 xv[5];
#pragma unroll
        for (int n = 0; n < 5; ++n) xv[n] = *(const GAS u32x2*)(x + xoff + n * 32);
        flag_poll<2>(ws, WS_FO + ((rb >> 2) * 32 + r32) * 128, tag);
        STAMPP(1, 9);
        u32x4 a0[4], a1[4], a2[4];
        auto loado = [&](int s, u32x4 (&a)[4]) {
#pragma unroll
            for (int i = 0; i < 4; ++i) a[i] = __builtin_amdgcn_raw_buffer_load_b128(wsr, lane * 16, aob + (48 + 5 * qq + s) * 1024 + ((qq + i) & 3) * (272 * 256), SC1);
        };
        loado(0, a0); loado(1, a1); loado(2, a2); SBAR();
        lds_bar();
        { const LAS f32x4* p = (const LAS f32x4*)(lds + L_KV0) + qq * 320 + lane;
#pragma unroll
          for (int n = 0; n < 5; ++n) {
              const f32x4 xf = {__uint_as_float(xv[n][0] << 16), __uint_as_float(xv[n][0] & 0xffff0000u), __uint_as_float(xv[n][1] << 16), __uint_as_float(xv[n][1] & 0xffff0000u)};
              acc[0][n] = (p[n * 64] + p[1280 + n * 64]) + (p[2560 + n * 64] + p[3840 + n * 64]) + xf;
              acc[1][n] = (f32x4){}; acc[2][n] = (f32x4){}; acc[3][n] = (f32x4){};
              if (n & 1) SBAR();
          } }
        SBAR();
#define WOF(s) {pre_frag<s>(pre), pre_frag<5 + s>(pre), pre_frag<10 + s>(pre), pre_frag<15 + s>(pre), pre_frag<20 + s>(pre)}
        { const u32x4 b[5] = WOF(0); mma(a0, b); } SBAR(); loado(3, a0); SBAR();
        { const u32x4 b[5] = WOF(1); mma(a1, b); } SBAR(); loado(4, a1); SBAR();
        { const u32x4 b[5] = WOF(2); mma(a2, b); }
        { const u32x4 b[5] = WOF(3); mma(a0, b); }
        { const u32x4 b[5] = WOF(4); mma(a1, b); }
        STAMPP(1, 10);
        auto red = [&](int r) { return (LAS f32x4*)(lds + (r >> 1) * L_RED2 + (r & 1) * 15360) + lane; };
#pragma unroll
        for (int i = 1; i < 4; ++i)
#pragma unroll
            for (int n = 0; n < 5; ++n) red((qq + i) & 3)[(i - 1) * 320 + n * 64] = acc[i][n];
        lds_bar();
#pragma unroll
        for (int n = 0; n < 5; ++n) {
            const LAS f32x4* p = red(qq) + n * 64;
            const f32x4 sum = (acc[0][n] + p[0]) + (p[320] + p[640]);
            *(GAS u32x2*)(out + xoff + n * 32) = (u32x2){pk2(sum[0], sum[1]), pk2(sum[2], sum[3])};
        }
    }
    STAMPP(1, 11); STAMPP(3, 11);
    STAMP_OUT;
}

extern "C" void kernel_launch(void* const* d_in, const int* in_sizes, int n_in, void* d_out, int out_size, void* d_ws, size_t ws_size,
                              hipStream_t stream) {
    fused_block<<<256, 512, 0, stream>>>((const char*)d_in[0], (const char*)d_in[1], (const char*)d_in[2], (const char*)d_in[4],
                                         (const char*)d_in[5], (const char*)d_in[6], (char*)d_out, (char*)d_ws);
}
```

```cpp
#include <hip/hip_runtime.h>
#include <hip/hip_bf16.h>

typedef __bf16 bf16x8 __attribute__((ext_vector_type(8)));
typedef __bf16 bf16x2 __attribute__((ext_vector_type(2)));
typedef short s16x4 __attribute__((ext_vector_type(4)));
typedef float f32x32 __attribute__((ext_vector_type(32)));
typedef float f32x16 __attribute__((ext_vector_type(16)));
typedef float f32x4 __attribute__((ext_vector_type(4)));
typedef unsigned u32x32 __attribute__((ext_vector_type(32)));
typedef unsigned u32x8 __attribute__((ext_vector_type(8)));
typedef unsigned u32x4 __attribute__((ext_vector_type(4)));
typedef unsigned u32x2 __attribute__((ext_vector_type(2)));
typedef unsigned long long u64;
#define LAS __attribute__((address_space(3)))
#define GAS __attribute__((address_space(1)))
#define DEV __device__ __forceinline__

constexpr unsigned WS_EPOCH = 0;
constexpr unsigned WS_FA = 4096;
constexpr unsigned WS_FH = 8192;
constexpr unsigned WS_FW = 12288;
constexpr unsigned WS_FO = 16384;
constexpr unsigned WS_GA = 65536;
constexpr unsigned WS_GB = WS_GA + 256 * 384 * 8;
constexpr unsigned WS_Q = WS_GB + 256 * 192 * 8;
constexpr unsigned KVS = 5120;
constexpr unsigned WS_KV = WS_Q + 16 * 64 * 2560;
constexpr unsigned WS_HP = WS_KV + 16 * 64 * KVS;
constexpr unsigned WS_WP = WS_HP + 128 * 272 * 256;
constexpr unsigned WS_END = WS_WP + 40 * 68 * 1024;
constexpr unsigned OOB = 0x40000000u;

constexpr int L_TR = 131072;
constexpr int L_PART = L_TR + 16384;
constexpr int L_FIN = L_PART + 6144;
constexpr int KVT = 2848;
constexpr int l_kv(int kh) { return kh * 65536; }
constexpr int l_bst(int w) { return 12 * KVT + (w >> 1) * 65536 + (w & 1) * 15360; }
constexpr int L_MRG = 136576;
constexpr int L_FLAG = L_MRG + 26624;
constexpr int L_TOTAL = 163840;

constexpr float QSCALE = 0.22360679774997896f * 1.4426950408889634f;
constexpr int SC1 = 16;
constexpr int NT0 = 32;
constexpr int CODE_LINES = 420;
constexpr unsigned SPIN_CAP = 1u << 22;

DEV int lane_id() {
    int l; asm volatile("v_mbcnt_lo_u32_b32 %0, -1, 0\n\tv_mbcnt_hi_u32_b32 %0, -1, %0" : "=v"(l)); return l;
}
DEV unsigned pk2(float lo, float hi) { bf16x2 v = {(__bf16)lo, (__bf16)hi}; return __builtin_bit_cast(unsigned, v); }
DEV float bfr(float x) { return (float)(__bf16)x; }
DEV u32x2 split3(float v) {
    const float a = bfr(v), b = bfr(v - a), c = v - a - b;
    return (u32x2){pk2(a, b), pk2(c, 0.f)};
}
DEV float mul1(float a, float b) { float r; asm("v_mul_f32 %0, %1, %2" : "=v"(r) : "v"(a), "v"(b)); return r; }
DEV float add1(float a, float b) { float r; asm("v_add_f32 %0, %1, %2" : "=v"(r) : "v"(a), "v"(b)); return r; }
DEV float fma1(float a, float b, float c) { float r; asm("v_fma_f32 %0, %1, %2, %3" : "=v"(r) : "v"(a), "v"(b), "v"(c)); return r; }
DEV float swap_add(float v) {
    auto r = __builtin_amdgcn_permlane32_swap(__float_as_uint(v), __float_as_uint(v), false, false);
    return __uint_as_float(r[0]) + __uint_as_float(r[1]);
}
DEV float swap_max(float v) {
    auto r = __builtin_amdgcn_permlane32_swap(__float_as_uint(v), __float_as_uint(v), false, false);
    return fmaxf(__uint_as_float(r[0]), __uint_as_float(r[1]));
}
template <int P> DEV void dma16(unsigned voff, u32x4 srd, unsigned soff, unsigned dst) {
    if constexpr (P == 0) asm volatile("s_mov_b32 m0, %3\n\ts_nop 0\n\tbuffer_load_dwordx4 %0, %1, %2 offen lds" ::"v"(voff), "s"(srd), "s"(soff), "s"(dst) : "memory");
    else asm volatile("buffer_load_dwordx4 %0, %1, %2 offen offset:%3 lds" ::"v"(voff), "s"(srd), "s"(soff), "i"(1024 * P) : "memory");
}
DEV u32x4 make_srd(const void* p, unsigned bytes) { return (u32x4){(unsigned)(size_t)p, (unsigned)((size_t)p >> 32) & 0xffffu, bytes, 0x00020000u}; }
DEV void glds4(unsigned voff, const void* sbase, unsigned ldsdst) {
    asm volatile("s_mov_b32 m0, %2\n\ts_nop 0\n\tglobal_load_lds_dword %0, %1 sc1" ::"v"(voff), "s"(sbase), "s"(ldsdst) : "memory");
}
template <int N> DEV void wait_bar() { __builtin_amdgcn_sched_barrier(0); asm volatile("s_waitcnt vmcnt(%0) lgkmcnt(0)\n\ts_barrier" ::"i"(N) : "memory"); __builtin_amdgcn_sched_barrier(0); }
DEV void lds_bar() { wait_bar<63>(); }
DEV bf16x8 lds128(const LAS char* p) { return *(const LAS bf16x8*)p; }
DEV bf16x8 ldstr(const LAS char* p0, const LAS char* p1) {
    s16x4 a = __builtin_amdgcn_ds_read_tr16_b64_v4i16((LAS s16x4*)p0), b = __builtin_amdgcn_ds_read_tr16_b64_v4i16((LAS s16x4*)p1);
    return __builtin_bit_cast(bf16x8, __builtin_shufflevector(a, b, 0, 1, 2, 3, 4, 5, 6, 7));
}
DEV float dot_ones(bf16x8 v, float acc, unsigned ones = 0x3f803f80u) {
    const bf16x2 one = __builtin_bit_cast(bf16x2, ones);
#pragma unroll
    for (int i = 0; i < 4; ++i) acc = __builtin_amdgcn_fdot2_f32_bf16((bf16x2){v[2 * i], v[2 * i + 1]}, one, acc, false);
    return acc;
}
DEV float dot_self(bf16x8 v, float acc) {
#pragma unroll
    for (int i = 0; i < 4; ++i) { bf16x2 p = {v[2 * i], v[2 * i + 1]}; acc = __builtin_amdgcn_fdot2_f32_bf16(p, p, acc, false); }
    return acc;
}
#define MFMA32(a, b, c) __builtin_amdgcn_mfma_f32_32x32x16_bf16(a, b, c, 0, 0, 0)
#define MFMA8(a, b, c) __builtin_amdgcn_mfma_f32_32x32x8bf16_1k(__builtin_bit_cast(s16x4, a), __builtin_bit_cast(s16x4, b), c, 0, 0, 0)
#define MFMA16(a, b, c) __builtin_amdgcn_mfma_f32_16x16x32_bf16(a, b, c, 0, 0, 0)
#define SBAR() __builtin_amdgcn_sched_barrier(0)


DEV u32x2 ld2_async(u32x4 srd, unsigned voff, unsigned soff) { u32x2 v; asm volatile("buffer_load_dwordx2 %0, %1, %2, %3 offen sc1" : "=v"(v) : "v"(voff), "s"(srd), "s"(soff) : "memory"); return v; }
DEV void flag_store(char* p, u64 tag) { __hip_atomic_store((GAS u64*)p, tag, __ATOMIC_RELAXED, __HIP_MEMORY_SCOPE_AGENT); }
DEV void gran_check(const char* ws, unsigned off, unsigned stride, u32x2 (&g)[4], unsigned tag) {
    if (__builtin_expect(__any(((g[0][1] ^ tag) | (g[1][1] ^ tag) | (g[2][1] ^ tag) | (g[3][1] ^ tag)) != 0u), 0)) {
#pragma unroll 1
        for (unsigned spin = 0; spin < SPIN_CAP; ++spin) {
            asm volatile("s_sleep 1\n\tglobal_load_dwordx2 %0, %4, %8 sc1\n\tglobal_load_dwordx2 %1, %5, %8 sc1\n\t"
                         "global_load_dwordx2 %2, %6, %8 sc1\n\tglobal_load_dwordx2 %3, %7, %8 sc1\n\ts_waitcnt vmcnt(0)"
                         : "=&v"(g[0]), "=&v"(g[1]), "=&v"(g[2]), "=&v"(g[3])
                         : "v"(off), "v"(off + stride), "v"(off + 2 * stride), "v"(off + 3 * stride), "s"(ws) : "memory");
            if (__all(g[0][1] == tag && g[1][1] == tag && g[2][1] == tag && g[3][1] == tag)) break;
        }
    }
}
DEV void flag_store_if0(u32x4 srd, unsigned off, u64 tag, int who) {
    asm volatile("s_cmp_lg_u32 %3, 0\n\ts_cbranch_scc1 1f\n\ts_mov_b64 exec, 1\n\tbuffer_store_dwordx2 %0, off, %1, %2 sc1\n\ts_mov_b64 exec, -1\n\t1:"
                 :: "v"(tag), "s"(srd), "s"(off), "s"(who) : "memory", "scc");
}
template <int SLEEP> DEV void flag_poll(const char* ws, unsigned off, u64 tag) {
#pragma unroll 1
    for (unsigned spin = 0; spin < SPIN_CAP; ++spin) {
        u64 f;
        asm volatile("global_load_dwordx2 %0, %1, %2 sc1\n\ts_waitcnt vmcnt(0)" : "=&v"(f) : "v"(off), "s"(ws) : "memory");
        if (__all(f == tag)) break;
        __builtin_amdgcn_s_sleep(SLEEP);
    }
}
DEV void flag_poll2(u32x4 srd, unsigned off, u64 tag, u64 (&f)[2]) {
    unsigned n;
    asm volatile("s_mov_b32 %2, 0x200000\n\tbuffer_load_dwordx2 %0, %3, %4, 0 offen sc1\n\ts_sleep 3\n\t"
                 "1:\n\tbuffer_load_dwordx2 %1, %3, %4, 0 offen sc1\n\ts_waitcnt vmcnt(1)\n\tv_cmp_ne_u64 vcc, %5, %0\n\ts_cbranch_vccz 2f\n\t"
                 "buffer_load_dwordx2 %0, %3, %4, 0 offen sc1\n\ts_waitcnt vmcnt(1)\n\tv_cmp_ne_u64 vcc, %5, %1\n\ts_cbranch_vccz 2f\n\t"
                 "s_sub_u32 %2, %2, 1\n\ts_cbranch_scc0 1b\n\t2:"
                 : "=&{v[250:251]}"(f[0]), "=&{v[252:253]}"(f[1]), "=&s"(n) : "v"(off), "s"(srd), "s"(tag) : "memory", "scc", "vcc");
}
DEV void lds_wait(LAS unsigned* word, unsigned n) {
#pragma unroll 1
    for (unsigned spin = 0; spin < SPIN_CAP; ++spin) {
        if (__builtin_amdgcn_readfirstlane(__hip_atomic_load(word, __ATOMIC_RELAXED, __HIP_MEMORY_SCOPE_WORKGROUP)) >= n) break;
        __builtin_amdgcn_s_sleep(1);
    }
    asm volatile("" ::: "memory");
}
DEV void flag_wait(const char* ws, unsigned off, u32x2 f, u64 tag) {
    if (__builtin_expect(__any(__builtin_bit_cast(u64, f) != tag), 0)) flag_poll<8>(ws, off, tag);
}
DEV void arrive_publish(LAS unsigned* cnt, unsigned last, int lane, char* flag, u64 tag) {
    if (lane == 0) {
        const unsigned old = __hip_atomic_fetch_add(cnt, 1u, __ATOMIC_RELAXED, __HIP_MEMORY_SCOPE_WORKGROUP);
        if (old == last) flag_store(flag, tag);
    }
}

#define EXP4(a, b, c, d) "v_exp_f32 v" #a ", v" #a "\n\tv_exp_f32 v" #b ", v" #b "\n\tv_exp_f32 v" #c ", v" #c "\n\tv_exp_f32 v" #d ", v" #d "\n\t"
#define E_S0_0 EXP4(64, 65, 66, 67)
#define E_S0_1 EXP4(68, 69, 70, 71)
#define E_S0_2 EXP4(72, 73, 74, 75)
#define E_S0_3 EXP4(76, 77, 78, 79)
#define E_S1_0 EXP4(80, 81, 82, 83)
#define E_S1_1 EXP4(84, 85, 86, 87)
#define E_S1_2 EXP4(88, 89, 90, 91)
#define E_S1_3 EXP4(92, 93, 94, 95)
#define E_S2_0 EXP4(96, 97, 98, 99)
#define E_S2_1 EXP4(100, 101, 102, 103)
#define E_S2_2 EXP4(104, 105, 106, 107)
#define E_S2_3 EXP4(108, 109, 110, 111)
#define E_S3_0 EXP4(112, 113, 114, 115)
#define E_S3_1 EXP4(116, 117, 118, 119)
#define E_S3_2 EXP4(120, 121, 122, 123)
#define E_S3_3 EXP4(124, 125, 126, 127)
#define R_S0 "v[64:79]"
#define R_S1 "v[80:95]"
#define R_S2 "v[96:111]"
#define R_S3 "v[112:127]"
#define R_S0L "v[64:71]"
#define R_S0H "v[72:79]"
#define R_S1L "v[80:87]"
#define R_S1H "v[88:95]"
#define R_S2L "v[96:103]"
#define R_S2H "v[104:111]"
#define R_S3L "v[112:119]"
#define R_S3H "v[120:127]"
#define R_O0 "v[128:143]"
#define R_O1 "v[144:159]"
#define R_V0A "v[160:163]"
#define R_V0B "v[164:167]"
#define R_V1A "v[168:171]"
#define R_V1B "v[172:175]"
#define R_K0M "v[176:179]"
#define R_K1M "v[180:183]"
#define R_K0R "v[184:185]"
#define R_K1R "v[186:187]"
#define R_Q0M "v[192:195]"
#define R_Q1M "v[196:199]"
#define R_Q0R "v[200:201]"
#define R_Q1R "v[202:203]"
#define QK1(s, k, q) "v_mfma_f32_32x32x16_bf16 " R_S##s ", " R_K##k##M ", " R_Q##q##M ", 0\n\t"
#define QK2(s, k, q) "v_mfma_f32_32x32x8_bf16 " R_S##s ", " R_K##k##R ", " R_Q##q##R ", " R_S##s "\n\t"
#define PV1(o, v, s) "v_smfmac_f32_32x32x32_bf16 " R_O##o ", " R_V##v##A ", " R_S##s##L ", v204\n\t"
#define PV2(o, v, s) "v_smfmac_f32_32x32x32_bf16 " R_O##o ", " R_V##v##B ", " R_S##s##H ", v204\n\t"
#define PERIOD(e, sn, k, q, sp, v, o) \
    QK1(sn, k, q) E_S##e##_0 PV1(o, v, sp) E_S##e##_1 QK2(sn, k, q) E_S##e##_2 PV2(o, v, sp) E_S##e##_3
#define PERIOD_NOQK(e, sp, v, o) E_S##e##_0 PV1(o, v, sp) E_S##e##_1 E_S##e##_2 PV2(o, v, sp) E_S##e##_3
#define PERIOD_NOPV(e, sn, k, q) QK1(sn, k, q) E_S##e##_0 E_S##e##_1 QK2(sn, k, q) E_S##e##_2 E_S##e##_3
#define TOFF(d, t) "((it_+" #d ")%%3)*11392+" #t "*2848"
#define RDK(k, d, t) "ds_read_b128 " R_K##k##M ", v188 offset:" TOFF(d, t) "\n\tds_read_b64 " R_K##k##R ", v189 offset:" TOFF(d, t) "\n\t"
#define RDV(v, d, t) "ds_read_b128 " R_V##v##A ", v190 offset:" TOFF(d, t) "\n\tds_read_b128 " R_V##v##B ", v191 offset:" TOFF(d, t) "\n\t"
#define LGK0 "s_waitcnt lgkmcnt(0)\n\t"
#define FETCH_A "s_add_u32 m0, %[lb], " TOFF(2, 0) "\n\ts_addk_i32 s60, 20480\n\tbuffer_load_dwordx4 v205, %[srd], s60 offen sc1 lds\n\t"
#define FETCH_B "buffer_load_dwordx4 v206, %[srd], s60 offen offset:1024 sc1 lds\n\t"
#define FETCH_C "s_mov_b32 exec_hi, 0xffff\n\tbuffer_load_dwordx4 v207, %[srd], s60 offen offset:2048 sc1 lds\n\ts_mov_b32 exec_hi, -1\n\t"
#define FETCH FETCH_A FETCH_B FETCH_C
#define ARRIVE(n) "s_waitcnt vmcnt(" n ")\n\tds_add_u32 v211, v209\n\t"
#define PEEK "ds_read_b32 v210, v208\n\t"
#define SYNC "1:\n\tv_cmp_gt_u32 vcc, 4*(it_+2), v210\n\ts_cbranch_vccz 2f\n\ts_sleep 1\n\t" PEEK LGK0 "s_branch 1b\n\t2:\n\t"
#define PE0 PERIOD(0, 2, 1, 0, 3, 1, 1)
#define PE1 PERIOD(1, 3, 1, 1, 0, 0, 0)
#define PE2 PERIOD(2, 0, 0, 0, 1, 0, 1)
#define PE3 PERIOD(3, 1, 0, 1, 2, 1, 0)
#define ITER_X(P0, na, F, e1, e2, e3) \
        LGK0 P0 RDK(0, 0, 2) RDV(1, 0, 1) PE1 \
        LGK0 ARRIVE(na) e1 PE2 RDK(1, 0, 3) RDV(0, 0, 2) PEEK PE3 \
        LGK0 e2 PE0 SYNC RDK(0, 1, 0) RDV(1, 0, 3) F PE1 \
        LGK0 e3 PE2 RDK(1, 1, 1) RDV(0, 1, 0) PE3 ".set it_, it_+1\n\t"
#define ITER_LAST(e0, e2, e3) \
        LGK0 e0 PE0 RDK(0, 0, 2) RDV(1, 0, 1) PE1 \
        LGK0 PE2 RDK(1, 0, 3) RDV(0, 0, 2) PE3 \
        LGK0 e2 PE0 RDV(1, 0, 3) PE1 \
        LGK0 e3 PERIOD_NOQK(2, 1, 0, 1) PERIOD_NOQK(3, 2, 1, 0) "s_nop 1\n\t" PV1(1, 1, 3) PV2(1, 1, 3) "s_nop 15\n\t"

struct Attn { f32x32 sa, sb, oo; u32x32 kv; u32x8 qm, con; u32x4 qr, srd; unsigned lb, so; };
#define ATTN_DMA(a) "{v[204:211]}"(a.con), [srd] "s"(a.srd), [lb] "s"(a.lb)
template <int P> DEV void attn_load_q(Attn& a, unsigned q, unsigned v) {
    if constexpr (P == 0) asm volatile("buffer_load_dwordx4 " R_Q0M ", %1, %2, %3 offen sc1" : "=&{v[192:199]}"(a.qm) : "v"(v), "s"(a.srd), "s"(q) : "memory");
    if constexpr (P == 1) asm volatile("buffer_load_dwordx4 " R_Q1M ", %1, %2, %3 offen offset:2560 sc1" : "+{v[192:199]}"(a.qm) : "v"(v), "s"(a.srd), "s"(q) : "memory");
    if constexpr (P == 2) asm volatile("buffer_load_dwordx2 " R_Q0R ", %1, %2, %3 offen sc1" : "=&{v[200:203]}"(a.qr) : "v"(v), "s"(a.srd), "s"(q) : "memory");
    if constexpr (P == 3) asm volatile("buffer_load_dwordx2 " R_Q1R ", %1, %2, %3 offen offset:2560 sc1" : "+{v[200:203]}"(a.qr) : "v"(v), "s"(a.srd), "s"(q) : "memory");
}
template <int ST, int P> DEV void attn_fetch(Attn& a) {
    if constexpr (P == 0) asm volatile(".set it_, %[i]\n\t" FETCH_A : "+{s60}"(a.so) : ATTN_DMA(a), [i] "i"(ST + 1) : "memory", "scc");
    if constexpr (P == 1) asm volatile(FETCH_B : "+{s60}"(a.so) : ATTN_DMA(a) : "memory");
    if constexpr (P == 2) asm volatile(FETCH_C : "+{s60}"(a.so) : ATTN_DMA(a) : "memory");
}
#define ZERO8(r) "v_mov_b32 v[" #r "], 0\n\tv_mov_b32 v[" #r "+1], 0\n\tv_mov_b32 v[" #r "+2], 0\n\tv_mov_b32 v[" #r "+3], 0\n\t" \
                 "v_mov_b32 v[" #r "+4], 0\n\tv_mov_b32 v[" #r "+5], 0\n\tv_mov_b32 v[" #r "+6], 0\n\tv_mov_b32 v[" #r "+7], 0"
template <int P> DEV void attn_init_o(Attn& a) {
    if constexpr (P == 0) asm volatile(ZERO8(128) : "={v[128:159]}"(a.oo));
    if constexpr (P == 1) asm volatile(ZERO8(136) : "+{v[128:159]}"(a.oo));
    if constexpr (P == 2) asm volatile(ZERO8(144) : "+{v[128:159]}"(a.oo));
    if constexpr (P == 3) asm volatile(ZERO8(152) : "+{v[128:159]}"(a.oo));
}
DEV void attn_arrive0(Attn& a) {
    asm volatile(ARRIVE("3") : "+{v[192:199]}"(a.qm), "+{v[200:203]}"(a.qr) : "{v[204:211]}"(a.con) : "memory");
}
DEV void attn_peek(Attn& a) { asm volatile(PEEK : "+{v[204:211]}"(a.con) :: "memory"); }
DEV void attn_first_reads(Attn& a) {
    asm volatile(".set it_, -1\n\ts_waitcnt lgkmcnt(2)\n\t" SYNC RDK(0, 1, 0) RDK(1, 1, 1) RDV(0, 1, 0)
                 : "+{v[160:191]}"(a.kv), "+{v[204:211]}"(a.con) :: "memory", "vcc");
}
template <int NOPS> DEV void attn_scores0(Attn& a) {
    asm volatile("s_waitcnt lgkmcnt(4)\n\ts_nop 1\n\t" QK1(0, 0, 0) QK1(1, 0, 1) QK2(0, 0, 0) QK2(1, 0, 1) "s_nop %[n]"
                 : "=&{v[64:95]}"(a.sa), "+{v[160:191]}"(a.kv) : "{v[192:199]}"(a.qm), "{v[200:203]}"(a.qr), [n] "i"(NOPS) : "memory");
}
#define PF1(r, v, p, o) "buffer_load_dwordx4 v[" #r ":" #r "+3], %[" #v "], %[srd], " #p " offen offset:" #o " sc1\n\t"
#define NX(p) "s_add_u32 " #p ", " #p ", 69632\n\t"
#define NXA "s_add_u32 s64, s64, 69632\n\ts_cmp_ge_u32 s64, s68\n\ts_cselect_b32 s67, 278528, 0\n\ts_sub_u32 s64, s64, s67\n\t"
#define PA3(r0, r1, r2) PF1(r0, v, s64, 0) PF1(r1, v, s64, 1024) PF1(r2, v, s64, 2048) NXA
#define PF3(r0, r1, r2, p) PF1(r0, v, p, 0) PF1(r1, v, p, 1024) PF1(r2, v, p, 2048) NX(p)
#define PF5(r0, r1, r2, r3, r4) PF1(r0, v, s65, 0) PF1(r1, v, s65, 1024) PF1(r2, v, s65, 2048) PF1(r3, v, s65, 3072) PF1(r4, v2, s65, 0) NX(s65)
#define ST3 "s_mov_b32 m0, s67\n\ts_addk_i32 s67, 3072\n\tbuffer_load_dwordx4 %[v], %[srd], s66 offen sc1 lds\n\tbuffer_load_dwordx4 %[v], %[srd], s66 offen offset:1024 sc1 lds\n\t" \
            "buffer_load_dwordx4 %[v], %[srd], s66 offen offset:2048 sc1 lds\n\t" NX(s66)
#define PUBLISH_H(n, o, lab) "s_waitcnt vmcnt(" n ")\n\ts_mov_b64 exec, 1\n\tds_add_rtn_u32 v210, v208, v209 offset:" #o "\n\t" LGK0 "v_readfirstlane_b32 s63, v210\n\t" \
            "s_cmp_eq_u32 s63, 7\n\ts_cbranch_scc0 " lab "f\n\tv_mov_b32 v8, s70\n\tv_mov_b32 v9, s71\n\tbuffer_store_dwordx2 v[8:9], off, %[srd], s69 sc1\n\ts_nop 1\n\t" \
            lab ":\n\ts_mov_b64 exec, -1\n\t"
#define POLL_H "buffer_load_dwordx2 v[0:1], %[vf], %[srd], 0 offen sc1\n\t"
#define POLL_W "s_movk_i32 s63, 12288\n\tbuffer_load_dwordx4 v[0:3], %[v], %[srd], s63 offen sc1\n\tbuffer_load_dwordx4 v[4:7], %[v], %[srd], s63 offen offset:1024 sc1\n\t"
#define CHECK_W(l0, l1) "s_mov_b32 s69, 0x100000\n\t" l0 ":\n\tv_cmp_ne_u64 vcc, s[70:71], v[0:1]\n\tv_cmp_ne_u64 s[72:73], s[70:71], v[2:3]\n\ts_or_b64 s[74:75], vcc, s[72:73]\n\t" \
            "v_cmp_ne_u64 vcc, s[70:71], v[4:5]\n\tv_cmp_ne_u64 s[72:73], s[70:71], v[6:7]\n\ts_or_b64 s[74:75], s[74:75], vcc\n\ts_or_b64 s[74:75], s[74:75], s[72:73]\n\t" \
            "s_cbranch_scc0 " l1 "f\n\ts_sleep 32\n\t" POLL_W "s_waitcnt vmcnt(0)\n\ts_sub_u32 s69, s69, 1\n\ts_cbranch_scc0 " l0 "b\n\t" l1 ":\n\t"
struct Pre { u32x32 a, b, c; u32x4 d; };
#define ATTN_OUT(a, p, sx) "+{v[64:95]}"(a.sa), "=&{v[96:127]}"(a.sb), "+{v[128:159]}"(a.oo), "+{v[160:191]}"(a.kv), "+{s60}"(a.so), "+{v[204:211]}"(a.con), \
          "=&{v[0:31]}"(p.a), "=&{v[32:63]}"(p.b), "=&{v[212:243]}"(p.c), "=&{v[244:247]}"(p.d), "+{s[64:71]}"(sx)
#define ATTN_IN(a) "{v[192:199]}"(a.qm), "{v[200:203]}"(a.qr), [srd] "s"(a.srd), [lb] "s"(a.lb)
#define ITER_FIRST ".set it_, 0\n\t" ITER_X(PERIOD_NOPV(0, 2, 1, 0), "6", , , , )
#define ITER_N ".rept %[n]\n\t" ITER_X(PE0, "0", FETCH, , , ) ".endr\n\t"
template <int KH> DEV void attn_main(Attn& a, Pre& p, u32x8 sx, unsigned voff, unsigned v2) {
    if constexpr (KH == 0) asm volatile(
        ITER_FIRST
        ITER_X(PE0, "3", FETCH, , POLL_W, ) PUBLISH_H("3", 8, "30") CHECK_W("39", "40")
        "38:\n\tds_read_b32 v210, v208 offset:4\n\t" LGK0 "v_cmp_gt_u32 vcc, 8, v210\n\ts_cbranch_vccnz 38b\n\t"
        ITER_X(PE0, "0", FETCH, PF3(16, 52, 228, s65), PF3(20, 56, 232, s65), PF3(24, 60, 236, s65))
        ITER_X(PE0, "3", FETCH, PF3(28, 212, 240, s65), PF3(32, 216, 244, s65), ST3)
        ITER_X(PE0, "3", FETCH, ST3, ST3, )
        ITER_N
        ITER_X(PE0, "0", FETCH, ST3, ST3 POLL_H, )
        "s_waitcnt vmcnt(3)\n\ts_mov_b32 s69, 0x400000\n\t"
        "33:\n\tv_cmp_ne_u64 vcc, s[70:71], v[0:1]\n\ts_cbranch_vccz 34f\n\ts_sleep 8\n\t" POLL_H
        "s_waitcnt vmcnt(0)\n\ts_sub_u32 s69, s69, 1\n\ts_cbranch_scc0 33b\n\t34:\n\t"
        ITER_X(PE0, "0", , PA3(0, 36, 220), PA3(4, 40, 224), PF1(8, v, s64, 0) PF1(44, v, s64, 1024) NXA)
        ITER_LAST(PF1(12, v, s64, 0) PF1(48, v, s64, 1024), , )
        : ATTN_OUT(a, p, sx) : ATTN_IN(a), [v] "v"(voff), [vf] "v"(v2), [n] "i"(NT0 / 4 - 8) : "memory", "scc", "vcc", "s63", "s72", "s73", "s74", "s75");
    else asm volatile(
        ITER_FIRST PUBLISH_H("0", 4, "35") "s_setprio 1\n\t"
        ITER_X(PE0, "0", FETCH, , POLL_W, ) "s_waitcnt vmcnt(3)\n\t" CHECK_W("39", "40")
        ITER_X(PE0, "0", FETCH, PF5(0, 20, 40, 60, 228), PF5(4, 24, 44, 212, 232), PF5(8, 28, 48, 216, 236))
        ITER_X(PE0, "5", FETCH, PF5(12, 32, 52, 220, 240), PF5(16, 36, 56, 224, 244), )
        ITER_N
        ITER_X(PE0, "0", , , , )
        ITER_LAST(, "v_mov_b32 v188, s64\n\tds_read_b32 v210, v188\n\t",
                  "41:\n\tv_cmp_eq_u32 vcc, 0, v210\n\ts_cbranch_vccz 42f\n\ts_sleep 2\n\tds_read_b32 v210, v188\n\t" LGK0 "s_branch 41b\n\t42:\n\t"
                  "v_add_u32 v188, s66, %[v]\n\tv_lshrrev_b32 v189, 1, %[v]\n\tv_add_u32 v189, s66, v189\n\t"
                  "ds_read_b64 v[184:185], v189 offset:6144\n\tds_read_b128 v[64:67], v188\n\tds_read_b128 v[68:71], v188 offset:1024\n\tds_read_b128 v[72:75], v188 offset:2048\n\t"
                  "ds_read_b128 v[76:79], v188 offset:3072\n\tds_read_b128 v[176:179], v188 offset:4096\n\tds_read_b128 v[180:183], v188 offset:5120\n\t") LGK0
        : ATTN_OUT(a, p, sx) : ATTN_IN(a), [v] "v"(voff), [v2] "v"(v2), [n] "i"((64 - NT0) / 4 - 6) : "memory", "scc", "vcc", "s63", "s72", "s73", "s74", "s75");
}
template <int I> DEV u32x4 pre_frag(const Pre& p) {
    if constexpr (I < 8) return (u32x4){p.a[4 * I], p.a[4 * I + 1], p.a[4 * I + 2], p.a[4 * I + 3]};
    else if constexpr (I < 16) return (u32x4){p.b[4 * I - 32], p.b[4 * I - 31], p.b[4 * I - 30], p.b[4 * I - 29]};
    else if constexpr (I < 24) return (u32x4){p.c[4 * I - 64], p.c[4 * I - 63], p.c[4 * I - 62], p.c[4 * I - 61]};
    else return p.d;
}
template <int O, class V> DEV float max16(const V& s) {
    float a = fmaxf(fmaxf(s[O], s[O + 1]), s[O + 2]), b = fmaxf(fmaxf(s[O + 3], s[O + 4]), s[O + 5]);
    a = fmaxf(fmaxf(a, s[O + 6]), s[O + 7]); b = fmaxf(fmaxf(b, s[O + 8]), s[O + 9]);
    a = fmaxf(fmaxf(a, s[O + 10]), s[O + 11]); b = fmaxf(fmaxf(b, s[O + 12]), s[O + 13]);
    a = fmaxf(fmaxf(a, s[O + 14]), s[O + 15]);
    return fmaxf(a, b);
}
typedef __bf16 bf16x16 __attribute__((ext_vector_type(16)));
typedef float f32x8 __attribute__((ext_vector_type(8)));
DEV bool attn_overflowed(const Attn& a) {
    float k[4] = {(a.oo[8] * 0x1p30f) * 0.f, (a.oo[24] * 0x1p30f) * 0.f, 0.f, 0.f};
#pragma unroll
    for (int i = 0; i < 24; ++i) k[i & 3] = fma1(a.oo[i < 12 ? i : i + 4], 0.f, k[i & 3]);
    return __any(!((k[0] + k[1]) + (k[2] + k[3]) == 0.f));
}
DEV void attn_redo(Attn& a, float (&c)[2], __amdgpu_buffer_rsrc_t wsr, unsigned kvoff, int odd, int ntiles) {
    {
        asm volatile("s_waitcnt vmcnt(0)" ::: "memory");
        const int lane = lane_id(), r32 = lane & 31, hh = lane >> 5;
        const unsigned vk = odd * 1536 + lane * 16, vr = hh ? OOB : 1024 + r32 * 16 + odd * 8, vv = r32 < 20 ? 2560 + odd * 1280 + hh * 320 + r32 * 16 : OOB;
        const unsigned one = r32 == 20 ? 0x3f803f80u : 0u;
        const u32x4 qm[2] = {{a.qm[0], a.qm[1], a.qm[2], a.qm[3]}, {a.qm[4], a.qm[5], a.qm[6], a.qm[7]}};
        const u32x2 qr[2] = {{hh ? 0u : a.qr[0], hh ? 0u : a.qr[1]}, {hh ? 0u : a.qr[2], hh ? 0u : a.qr[3]}};
        f32x16 o[2] = {};
        c[0] = -INFINITY; c[1] = -INFINITY;
#pragma unroll 1
        for (int pass = 0; pass < 2; ++pass) {
#pragma unroll 1
            for (int t = 0; t < ntiles; ++t) {
                const u32x4 km = __builtin_amdgcn_raw_buffer_load_b128(wsr, vk, kvoff + t * KVS, SC1);
                const u32x2 kr = __builtin_amdgcn_raw_buffer_load_b64(wsr, vr, kvoff + t * KVS, SC1);
                const u32x4 va = __builtin_amdgcn_raw_buffer_load_b128(wsr, vv, kvoff + t * KVS, SC1) | one;
                const u32x4 vb = __builtin_amdgcn_raw_buffer_load_b128(wsr, vv + 640, kvoff + t * KVS, SC1) | one;
#pragma unroll
                for (int i = 0; i < 2; ++i) {
                    f32x16 s = MFMA32(__builtin_bit_cast(bf16x8, km), __builtin_bit_cast(bf16x8, qm[i]), (f32x16){});
                    s = MFMA8(kr, qr[i], s);
                    if (pass == 0) c[i] = fmaxf(c[i], max16<0>(s));
                    else {
                        f32x8 pl, ph;
#pragma unroll
                        for (int e = 0; e < 8; ++e) { pl[e] = __builtin_amdgcn_exp2f(s[e] - c[i]); ph[e] = __builtin_amdgcn_exp2f(s[8 + e] - c[i]); }
                        o[i] = __builtin_amdgcn_smfmac_f32_32x32x32_bf16(__builtin_bit_cast(bf16x8, va), __builtin_bit_cast(bf16x16, pl), o[i], 0xdddd, 0, 0);
                        o[i] = __builtin_amdgcn_smfmac_f32_32x32x32_bf16(__builtin_bit_cast(bf16x8, vb), __builtin_bit_cast(bf16x16, ph), o[i], 0xdddd, 0, 0);
                    }
                }
            }
            c[0] = swap_max(c[0]); c[1] = swap_max(c[1]);
        }
#pragma unroll
        for (int e = 0; e < 16; ++e) { a.oo[e] = o[0][e]; a.oo[16 + e] = o[1][e]; }
    }
}

DEV int rot10(int s0, int c) { const int cc = s0 + (c == 2 ? 3 : (c == 3 ? 2 : c)); return cc >= 10 ? cc - 10 : cc; }

template <int V> struct IC { static constexpr int value = V; };
#define PIN(x) asm volatile("" : "+v"(x))
struct Fetch1 { u32x4 srd; unsigned vo[4], base; };
DEV void fetch1_lanes(Fetch1& f, int kh, int lane) {
    const int step = (kh ? 8 * 1280 : 4 * 3840) - 1024;
    f.vo[0] = (lane >> (4 - kh)) * (kh ? 1280 : 3840) + (((lane & (15 >> kh)) ^ ((lane >> 4) << (2 - 2 * kh))) << 4);
    f.vo[1] = (f.vo[0] ^ (kh << 6)) + step; f.vo[2] = f.vo[0] + 2 * step; f.vo[3] = f.vo[1] + 2 * step;
}
DEV unsigned fetch1_off(int kh, int qq, int m, int j) { return (kh ? m : min(j, 14)) * (kh ? 128 * 1280 : 256) + qq * (kh ? 32 * 1280 : 16 * 3840); }
DEV int first_chunk(int mi, int ji) { const int v = ((5 * ji + 1) >> 1) + mi + (mi >> 2) + ((blockIdx.x >> 1) & 1); return v >= 10 ? v - 10 : v; }
template <int KH> DEV void program(const char* x, const char* wqkv, const char* wo, const char* wg, const char* wu, const char* wd, char* out, char* ws,
                                   LAS char* lds, const int w) {
    const unsigned lds0 = (unsigned)(size_t)lds;
    const int bid = blockIdx.x, xcd = bid & 7, rr = bid >> 3, qq = w & 3;
#define LANE_IDS const int lane = lane_id(); \
    const int tid = 64 * w + lane, r32 = lane & 31, hh = lane >> 5; (void)tid; (void)r32; (void)hh
    const __amdgpu_buffer_rsrc_t wsr = __builtin_amdgcn_make_buffer_rsrc(ws, 0, WS_END, 0x00020000);
    LAS unsigned* lflag = (LAS unsigned*)(lds + L_FLAG);
    u64 tag;
    const int mi = rr & 7, ji = rr >> 3, m = 8 * (xcd & 1) + mi, j = 4 * (xcd >> 1) + ji;
    const int g = 4 * xcd + (rr >> 3), mq = rr & 7, odd = g & 1;
    const unsigned kvoff = WS_KV + ((g >> 1) * 64 + NT0 * KH) * KVS;
    Pre pre;
    const int rb = 4 * xcd + (rr >> 3), ct = rr & 7;
    const unsigned aob = WS_HP + 4 * rb * (272 * 256), bob = WS_WP + 5 * ct * (68 * 1024);
    const int krot = 3 * ((ct + (rr >> 3)) & 3);
    u32x8 sx;
    unsigned om, orm;
    Attn at;
    at.srd = make_srd(ws, WS_END);
    float c[2];
    {
        LANE_IDS;
        const int s0 = first_chunk(mi, ji);
        Fetch1 f1; fetch1_lanes(f1, KH, lane); f1.base = fetch1_off(KH, qq, m, j); f1.srd = make_srd(KH ? x : wqkv, KH ? 2048 * 1280 : 640 * 3840);
        asm volatile("" : "+v"(f1.vo[0]), "+v"(f1.vo[1]), "+v"(f1.vo[2]), "+v"(f1.vo[3]));
        const u32x4 srd2 = make_srd(KH ? wu : wg, 640 * 3072);
        unsigned go[3], sof = 0;
#define PC(c, p) do { if constexpr ((c) < 10) { if constexpr ((p) == 0) sof = f1.base + rot10(s0, c) * (KH ? 128 : 64 * 3840); \
            dma16<p>(f1.vo[p], f1.srd, sof, lds0 + (1 - KH) * 65536 + ((c) & 3) * 16384 + qq * 4096); \
        } else if constexpr ((c) < 20 && (p) < 3) { if constexpr ((p) == 0) sof = j * 192 + 16 * qq * 3072 + rot10(s0, (c) % 10) * (64 * 3072); \
            dma16<p>(go[(p) % 3], srd2, sof, lds0 + KH * (65536 + 32) + ((c) & 3) * 16384 + qq * 3072); } } while (0)
#define PALL(c) do { PC(c, 0); PC(c, 1); PC(c, 2); PC(c, 3); } while (0)
        asm volatile("s_load_dwordx2 %0, %1, 0x0" : "=s"(tag) : "s"(ws) : "memory");
        unsigned tag32;
        if (!KH && tid < 16) lflag[tid] = 0u;

        const int g4 = lane >> 4, q4 = (lane >> 2) & 3, p4 = lane & 3, rot = qq ^ (2 * KH), xrow = 32 * qq + r32;
        unsigned wadr[2][4], xadr[4];
#pragma unroll
        for (int k = 0; k < 4; ++k) {
            xadr[k] = (xrow * 128 + ((hh ^ ((xrow >> 1) & 7)) << 4)) ^ ((k ^ rot) << 5);
#pragma unroll
            for (int b = 0; b < 2; ++b)
                wadr[b][k] = 65536 + ((k ^ rot) << 12) + (8 * (g4 >> 1) + q4) * 256 + (g4 & 1) * 32 + (p4 & 1) * 16 + (p4 >> 1) * 8 + (((2 * KH + b) ^ q4) << 6);
        }
        asm volatile("" : "+v"(xadr[0]), "+v"(xadr[1]), "+v"(xadr[2]), "+v"(xadr[3]), "+v"(wadr[0][0]), "+v"(wadr[0][1]), "+v"(wadr[0][2]), "+v"(wadr[0][3]),
                          "+v"(wadr[1][0]), "+v"(wadr[1][1]), "+v"(wadr[1][2]), "+v"(wadr[1][3]));
        LAS float* part = (LAS float*)(lds + L_PART); LAS u32x2* fin = (LAS u32x2*)(lds + L_FIN);
        bf16x8 xf[40], fw[2][3];
        f32x16 acc[2] = {};
        float rs = 0.f, rq = 0.f, r2, q2, cs[3] = {}, pv[4], rstd, st0, st1, ta, tb;
        unsigned nmu, l2, t0, t1, t2; u32x2 gr[4], fa, cf[3]; bf16x8 pk[2];
        const unsigned col2 = (mi & 3) == 3 ? 0x3f803f80u : 0u;
        LAS float *pw_row, *pw_col, *pr;
        unsigned pub_o, sto[4], pr0, pr1, pr2, pdst, prh, qof, hb;
        u32x4 psrd;
        const unsigned gab = KH ? WS_GA + (xcd + 64 * ji + 32 * (qq >> 1)) * 3072 + 2048 + (qq & 1) * 512 : WS_GA + (xcd + 8 * mi) * 3072 + qq * 512;
        constexpr unsigned gas = KH ? 8 * 3072 : 64 * 3072;
        const int gbh = w >= 3 && w < 6;
        const unsigned gbb = WS_GB + (xcd + 64 * ji + 32 * gbh) * 1536 + (w < 6 ? w - 3 * gbh : 0) * 512, ring = lds0 + l_kv(KH);
        auto setup2 = [&]() {
#pragma unroll
            for (int i = 0; i < 3; ++i) { const int p = 64 * i + lane; go[i] = (p / 12) * 3072 + (p % 12) * 16 - 1024 * i; }
            pw_row = part + (2 * KH + hh) * 384 + xrow; pw_col = part + qq * 384 + 256 + 64 * KH + lane; pr = part + (w < 6 ? tid : lane);
            pub_o = tid * 8 + (w < 6 ? WS_GA + bid * 3072 : OOB);
            const int tile = 4 * m + qq;
            if (j < 10) {
#pragma unroll
                for (int i = 0; i < 4; ++i) {
                    const int ci = (j < 5 ? j : j - 5) * 16 + 8 * KH + hh + 2 * i, pn = ci / 5, pl = ci - 5 * pn;
                    sto[i] = (j < 5 ? WS_Q : WS_KV) + (pn * 64 + tile) * (j < 5 ? 2560 : KVS) + pl * 512 + r32 * 16;
                }
            } else {
#pragma unroll
                for (int i = 0; i < 4; ++i) {
                    const int col = (j - 10) * 128 + 64 * KH + 32 * (i >> 1) + r32, head = col / 20, d = col - 20 * head;
                    sto[i] = (j < 15 ? WS_KV : OOB) + ((head >> 1) * 64 + tile) * KVS + 2560 + (head & 1) * 1280 + d * 16 + hh * 320 + (i & 1) * 640;
                }
            }
            asm volatile("" : "+v"(pw_row), "+v"(pw_col), "+v"(pr), "+v"(pub_o), "+v"(go[0]), "+v"(go[1]), "+v"(go[2]), "+v"(sto[0]), "+v"(sto[1]), "+v"(sto[2]), "+v"(sto[3]));
            if constexpr (!KH) {
                const int unit = bid + 256 * qq, us = unit / 10, nb = unit - 10 * us;
                psrd = make_srd(us < 48 ? wd : wo, unit < 680 ? (us < 48 ? 1536u * 1280u : 640u * 1280u) : 0u);
                pr0 = (us < 48 ? 32 * us : (us < 64 ? 40 * (us - 48) : 160 * (us - 64) + 16)) * 1280 + nb * 128; pr1 = (us < 64 ? 4 : 40) * 1280; pr2 = (us < 48 ? 16 : (us < 64 ? 20 : 80)) * 1280;
                pdst = unit < 680 ? WS_WP + (4 * nb * 68 + us) * 1024 : OOB; prh = (us < 64 ? 8 : 20) * 1280;
                asm volatile("" : "+s"(psrd), "+s"(pr0), "+s"(pr1), "+s"(pr2), "+s"(pdst), "+s"(prh));
            }
            qof = WS_Q + ((g >> 1) * 64 + 8 * mq + 2 * qq) * 2560; hb = WS_HP + (8 * m + 2 * qq) * (272 * 256) + 2 * (6 * j + 3 * KH) * 256;
            asm volatile("" : "+s"(qof), "+s"(hb));
        };
#define RW1(f, b, c, k) fw[f][b] = ldstr(lds + ((c) & 3) * 16384 + wadr[b][k], lds + ((c) & 3) * 16384 + wadr[b][k] + 1024)
#define RX1(c, k) xf[4 * (c) + (k)] = lds128(lds + ((c) & 3) * 16384 + xadr[k])
#define M1(f, b, c, k) acc[b] = MFMA32(fw[f][b], xf[4 * (c) + (k)], acc[b]); SBAR()
#define PART4(p) do { pv[0] = (p)[0]; pv[1] = (p)[384]; pv[2] = (p)[768]; pv[3] = (p)[1152]; } while (0)
#define ROWD(i, S, Q) do { S = dot_ones(xf[i], S); Q = dot_self(xf[i], Q); S = dot_ones(xf[i + 1], S); Q = dot_self(xf[i + 1], Q); PIN(S); PIN(Q); } while (0)
#define COLD(n, one) do { _Pragma("unroll") for (int b = 0; b < n; ++b) { cs[b] = dot_ones(fw[0][b], cs[b], one); PIN(cs[b]); } } while (0)
#define AT(cc, ss) if constexpr (c == cc && s == ss)
#define GSUM add1(add1(__uint_as_float(gr[0][0]), __uint_as_float(gr[1][0])), add1(__uint_as_float(gr[2][0]), __uint_as_float(gr[3][0])))
        float (&cr)[2] = c;
        auto job = [&](auto C_, auto S_) {
            constexpr int c = decltype(C_)::value, s = decltype(S_)::value;
            AT(0, 7) { ROWD(0, rs, rq); COLD(2, 0x3f803f80u); }
            AT(1, 7) { ROWD(4, rs, rq); COLD(2, col2); }
            AT(2, 2) { cs[0] = swap_add(cs[0]); PIN(cs[0]); }
            AT(2, 3) { cs[1] = swap_add(cs[1]); PIN(cs[1]); }
            AT(2, 4) { *pw_col = hh ? cs[1] : cs[0]; }
            AT(3, 3) { r2 = rs; q2 = rq; ROWD(12, r2, q2); }
            AT(3, 4) { pw_row[0] = (ji & 1) ? rs : r2; pw_row[128] = (ji & 1) ? rq : q2; }
            AT(4, 6) { if constexpr (KH) glds4((bid * 60 + (qq & 1) * 30 + min(lane, 29)) * 128, qq < 2 ? wg : wu, lds0 + L_TR + qq * 256); }
            AT(4, 0) { PART4(pr); }
            AT(4, 4) { st0 = add1(add1(pv[0], pv[1]), add1(pv[2], pv[3])); PIN(st0); }
            AT(4, 5) { __builtin_amdgcn_raw_buffer_store_b64((u32x2){__float_as_uint(st0), tag32}, wsr, pub_o, 0, SC1); }
            AT(6, 5) { l2 = lane_id() * 8; PIN(l2); }
            if constexpr (c == 7 && s < 4) gr[s] = ld2_async(at.srd, l2, gab + s * gas);
            if constexpr (c == 7 && s >= 4 && s < 7) PC(10, s - 4);
            AT(8, 0) { asm volatile("s_waitcnt vmcnt(3)" : "+v"(gr[0]), "+v"(gr[1]), "+v"(gr[2]), "+v"(gr[3]) :: "memory"); gran_check(ws, gab + l2, gas, gr, tag32); }
            AT(8, 1) { st0 = GSUM; if constexpr (KH) { ta = bfr(st0); PIN(ta); } PIN(st0); }
            AT(8, 2) { if constexpr (KH) { st0 -= ta; tb = bfr(st0); st0 -= tb; PIN(tb); PIN(st0); } }
            AT(8, 3) { if constexpr (KH) *(LAS u32x2*)((LAS char*)(fin + 256 + (qq >> 1) * 192 + (qq & 1) * 64) + l2) = (u32x2){pk2(ta, tb), pk2(st0, 0.f)};
                       else *(LAS float*)((LAS char*)(fin + 64 * qq) + l2) = st0; }
            AT(8, 5) { t0 = l2 & 255; t1 = t0 + (l2 >> 8) * 1536; PIN(t0); PIN(t1); }
            AT(9, 0) { st0 = *(const LAS float*)((LAS char*)(fin + 32 * qq) + t0); st1 = *(const LAS float*)((LAS char*)(fin + 128 + 32 * qq) + t0);
                       cf[0] = *(const LAS u32x2*)((LAS char*)(fin + 256 + 64 * KH) + t1); cf[1] = *(const LAS u32x2*)((LAS char*)(fin + 288 + 64 * KH) + t1); }
            AT(9, 3) { st0 *= 1.f / 640.f; PIN(st0); st1 = fmaxf(st1 * (1.f / 640.f) - st0 * st0, 0.f); nmu = pk2(-st0, -st0); PIN(st1); PIN(nmu); }
            AT(9, 4) { st1 = bfr(bfr(st1) + 1e-5f); PIN(st1); }
            AT(9, 5) { rstd = __builtin_amdgcn_rcpf(bfr(__builtin_amdgcn_sqrtf(st1))); PIN(rstd); }

            AT(10, 11) { COLD(3, 0x3f803f80u); }
            AT(11, 3) { COLD(3, col2); }
            if constexpr (c == 11 && s >= 4 && s < 7) { cs[s - 4] = swap_add(cs[s - 4]); PIN(cs[s - 4]); }
            AT(11, 7) { l2 = lane_id(); part[qq * 384 + 96 * KH + l2] = l2 >= 32 ? cs[1] : cs[0]; }
            AT(11, 8) { part[qq * 384 + 96 * KH + 64 + (l2 & 31)] = cs[2]; }
            AT(12, 0) { if constexpr (!KH) PART4(part + (qq < 3 ? 64 * qq : 0) + l2); }
            AT(12, 5) { if constexpr (!KH) { st0 = add1(add1(pv[0], pv[1]), add1(pv[2], pv[3])); PIN(st0); } }
            AT(12, 6) { if constexpr (!KH) __builtin_amdgcn_raw_buffer_store_b64((u32x2){__float_as_uint(st0), tag32}, wsr, l2 * 8 + (qq < 3 ? WS_GB + bid * 1536 + qq * 512 : OOB), 0, SC1); }
            AT(13, 0) { if constexpr (KH) flag_store_if0(at.srd, WS_FA + bid * 8, tag, qq); }
            AT(13, 4) { if constexpr (!KH) { t0 = (l2 >> 5) * prh + ((l2 >> 3) & 3) * 1280 + (l2 & 7) * 16; PIN(t0); } }
            if constexpr (c == 13 && s >= 5 && s < 9 && !KH) dma16<0>(t0, psrd, pr0 + ((s - 5) & 1) * pr1 + ((s - 5) >> 1) * pr2, lds0 + L_TR + w * 4096 + (s - 5) * 1024);
            AT(15, 4) { t2 = l2 * 8; PIN(t2); }
            if constexpr (c == 15 && s >= 5 && s < 9) gr[s - 5] = ld2_async(at.srd, t2, gbb + (s - 5) * (8 * 1536));
            AT(16, 4) { t0 = odd * 1024u + l2 * 16; t1 = 1024u + odd * 1280 + l2 * 16; PIN(t0); PIN(t1); }
            AT(16, 5) { at.lb = ring + qq * KVT; at.so = kvoff + (qq - 4) * KVS;
                        at.con = (u32x8){0xddddu, t0, l2 >= 32 ? t1 : t0, t1, lds0 + L_FLAG + 4 * KH, 1u, 0u, l2 == 0 ? lds0 + L_FLAG + 4 * KH : 0x00ffff00u}; asm volatile("" : "+{v[204:211]}"(at.con)); }
            AT(16, 10) { if constexpr (!KH) fa = ld2_async(at.srd, t2, WS_FA + qq * 512); }
            AT(17, 0) { asm volatile("s_waitcnt vmcnt(%4)" : "+v"(gr[0]), "+v"(gr[1]), "+v"(gr[2]), "+v"(gr[3]) : "i"(KH ? 3 : 4) : "memory"); gran_check(ws, gbb + t2, 8 * 1536, gr, tag32); }
            AT(17, 1) { st0 = GSUM; ta = bfr(st0); PIN(ta); PIN(st0); }
            AT(17, 2) { st0 -= ta; tb = bfr(st0); st0 -= tb; PIN(tb); PIN(st0); }
            AT(17, 3) { *(LAS u32x2*)((LAS char*)(fin + 256 + (w < 6 ? 64 * w : 0)) + t2) = (u32x2){pk2(ta, tb), pk2(st0, 0.f)}; }
            if constexpr (c == 16 && s >= 6 && s < 10) attn_init_o<s - 6>(at);
            AT(17, 4) { t0 = (t2 & 255) + (t2 >> 8) * 1536; PIN(t0);
                        if constexpr (!KH) { t1 = L_TR + w * 4096 + (8 * (l2 >> 4) + ((l2 >> 2) & 3)) * 128 + (l2 & 3) * 8; PIN(t1); } }
            if constexpr (c == 17 && s >= 5 && s < 10 && !KH) {
                if constexpr (s >= 6) __builtin_amdgcn_raw_buffer_store_b128(__builtin_bit_cast(u32x4, pk[s & 1]), wsr, pdst + l2 * 16 + (s - 6) * (68 * 1024), 0, SC1);
                if constexpr (s == 5) pk[0] = ldstr(lds + t1, lds + t1 + 512);
                if constexpr (s < 8) pk[s & 1] = ldstr(lds + t1 + 32 * (s - 4), lds + t1 + 512 + 32 * (s - 4));
            }
            AT(17, 10) { if constexpr (!KH) { asm volatile("s_waitcnt vmcnt(4)" : "+v"(fa) :: "memory"); flag_wait(ws, WS_FA + qq * 512 + t2, fa, tag); } }
            AT(18, 0) { const LAS char* p = (LAS char*)(fin + 256 + 96 * KH) + t0; cf[0] = *(const LAS u32x2*)p; cf[1] = *(const LAS u32x2*)(p + 256); cf[2] = *(const LAS u32x2*)(p + 512);
                        if constexpr (KH) flag_store_if0(at.srd, WS_EPOCH, tag, bid + qq); }
            if constexpr (c == 18 && s >= 1 && s < 4) attn_fetch<0, s - 1>(at);
            AT(18, 3) { t0 = odd * 1536 + l2 * 16; t1 = l2 >= 32 ? OOB : 1024 + l2 * 16 + odd * 8; PIN(t0); PIN(t1); }
            if constexpr (c == 18 && s >= 4 && s < 8) attn_load_q<s - 4>(at, qof, s < 6 ? t0 : t1);
            if constexpr (c == 18 && s >= 8 && s < 11) attn_fetch<1, s - 8>(at);
            AT(18, 8) { t0 = ring + 1536 + (l2 >> 5) * 320 + (l2 & 31) * 16; t1 = ring + ((l2 & 31) == 20 ? 2816 : 2832); PIN(t0); PIN(t1); }
            AT(18, 9) { at.kv[28] = ring + odd * 512 + l2 * 16; at.kv[29] = ring + (l2 >= 32 ? 2816 : (odd ? 8 : 1024) + l2 * 16);
                        at.kv[30] = (l2 & 31) < 20 ? t0 : t1; at.kv[31] = (l2 & 31) < 20 ? t0 + 640 : t1; asm volatile("" : "+{v[160:191]}"(at.kv)); }
            AT(18, 10) { const unsigned l3 = min(l2, 23u); t0 = l_kv(KH) + ((l3 >> 3) * 4 + qq) * KVT + 2816 + (l3 & 7) * 4; t1 = (l3 & 4) ? 0u : 0x3f803f80u; PIN(t0); PIN(t1); }
            AT(14, 5) { sx = (u32x8){KH ? lds0 + L_FLAG + 4 * w : aob + (12 * qq + krot) * 1024 + qq * (272 * 256), bob + (KH ? 48 + 4 * qq : 12 * qq + krot) * 1024,
                          KH ? lds0 + L_MRG + qq * 6656 : bob + (12 * qq + (krot + 9) % 12) * 1024,
                          lds0 + l_bst(qq), aob + 4 * (272 * 256), WS_FH + (m * 16 + j) * 8, (unsigned)tag, (unsigned)(tag >> 32)};
                        asm volatile("" : "+{s[64:71]}"(sx)); }
            AT(19, 0) { *(LAS unsigned*)(lds + t0) = t1; }
            AT(19, 1) { t2 = hb + ((l2 >> 4) & 1) * (272 * 256) + (l2 >> 5) * 256 + (l2 & 15) * 16; PIN(t2); }
            AT(19, 3) { attn_arrive0(at); }
            if constexpr (c == 19 && s >= 4 && s < 7) attn_fetch<2, s - 4>(at);
            AT(19, 5) { attn_peek(at); }
            AT(19, 6) { attn_first_reads(at); }
            AT(19, 7) { if constexpr (!KH) arrive_publish(lflag + 9, 3u, l2, ws + WS_FW + bid * 8, tag); }
            AT(19, 8) { attn_scores0<7>(at); }
            AT(19, 10) { asm volatile("" : "+{v[64:95]}"(at.sa)); cr[0] = max16<0>(at.sa); PIN(cr[0]); }
            AT(19, 11) { cr[1] = max16<16>(at.sa); PIN(cr[1]); }
            AT(19, 12) { cr[0] = swap_max(cr[0]); cr[1] = swap_max(cr[1]); PIN(cr[0]); PIN(cr[1]); }
            if constexpr (c == 19 && (s == 13 || s == 14)) {
                const u32x2 t = split3(-cr[s - 13]);
                at.qr[2 * s - 26] = l2 >= 32 ? t[0] : at.qr[2 * s - 26]; at.qr[2 * s - 25] = l2 >= 32 ? t[1] : at.qr[2 * s - 25];
                asm volatile("" : "+{v[200:203]}"(at.qr));
            }
        };
#define J(c, s) job(IC<c>{}, IC<s>{}); SBAR()
#define A1I(c, n) do { \
        if constexpr (c > 0) { M1(1, 0, (c > 0 ? c - 1 : 0), 3); } \
        RW1(0, 0, c, 0); RX1(c, 0); SBAR(); RW1(0, 1, c, 0); SBAR(); RX1(c, 1); RW1(1, 0, c, 1); J(c, 0); \
        if constexpr (c > 0) { M1(1, 1, (c > 0 ? c - 1 : 0), 3); } \
        RW1(1, 1, c, 1); PC(n, 0); J(c, 1); \
        M1(0, 0, c, 0); RW1(0, 0, c, 2); RX1(c, 2); PC(n, 1); J(c, 2); \
        M1(0, 1, c, 0); RW1(0, 1, c, 2); PC(n, 2); J(c, 3); \
        M1(1, 0, c, 1); RW1(1, 0, c, 3); RX1(c, 3); PC(n, 3); J(c, 4); \
        M1(1, 1, c, 1); RW1(1, 1, c, 3); J(c, 5); \
        M1(0, 0, c, 2); J(c, 6); M1(0, 1, c, 2); J(c, 7); } while (0)
        SBAR();
        sof = f1.base + rot10(s0, 1) * (KH ? 128 : 64 * 3840);
        if constexpr (KH) { setup2(); SBAR(); }
        PC(1, 2); PC(1, 3);
        if constexpr (!KH) { setup2(); SBAR(); }
        wait_bar<5>();
        asm volatile("" : "+s"(tag));
        tag += 1; tag32 = (unsigned)tag;
        A1I(0, 2); PALL(3); wait_bar<8>();
        A1I(1, 4); wait_bar<8>();
        A1I(2, 5); wait_bar<8>();
        A1I(3, 6); wait_bar<8>();
        A1I(4, 7); wait_bar<9 + KH>();
        A1I(5, 8); wait_bar<9 + KH>();
        A1I(6, 9); wait_bar<9 + KH>();
        A1I(7, 20); wait_bar<11>();
        A1I(8, 11); wait_bar<10>();
        A1I(9, 12); wait_bar<6>();
        M1(1, 0, 9, 3); PC(13, 0); SBAR(); M1(1, 1, 9, 3); PC(13, 1); SBAR();
        acc[0] = MFMA8(cf[0], ((u32x2){nmu, nmu}), acc[0]); acc[1] = MFMA8(cf[1], ((u32x2){nmu, nmu}), acc[1]); SBAR(); PC(13, 2); SBAR();
        asm volatile("" : "+{v[64:67]}"(xf[28]), "+{v[68:71]}"(xf[29]), "+{v[72:75]}"(xf[30]), "+{v[76:79]}"(xf[31]), "+{v[80:83]}"(xf[32]), "+{v[84:87]}"(xf[33]),
                          "+{v[88:91]}"(xf[34]), "+{v[92:95]}"(xf[35]), "+{v[96:99]}"(xf[36]), "+{v[100:103]}"(xf[37]), "+{v[104:107]}"(xf[38]), "+{v[108:111]}"(xf[39]));
        { const float sc = j < 5 ? rstd * QSCALE : rstd;
          u32x4 y[4];
#pragma unroll
          for (int i = 0; i < 4; ++i)
#pragma unroll
              for (int e = 0; e < 4; ++e) y[i][e] = pk2(sc * acc[i >> 1][8 * (i & 1) + 2 * e], sc * acc[i >> 1][8 * (i & 1) + 2 * e + 1]);
          if (j >= 10) {
              LANE_IDS;
              const int q4 = (lane >> 2) & 3, p4 = lane & 3;
              LAS char* tp = lds + (KH ? L_TR + qq * 4096 : qq * 16384 + 12288);
#pragma unroll
              for (int i = 0; i < 4; ++i) *(LAS u32x4*)(tp + (i >> 1) * 2048 + r32 * 64 + (((2 * (i & 1) + hh) ^ ((r32 >> 1) & 3)) << 4)) = y[i];
              const int cu = 2 * ((lane >> 4) & 1) + (p4 >> 1);
              const LAS char* u0 = tp + (8 * hh + q4) * 64 + ((cu ^ (q4 >> 1)) << 4) + (p4 & 1) * 8;
              const LAS char* u1 = tp + (8 * hh + q4 + 4) * 64 + ((cu ^ (2 + (q4 >> 1))) << 4) + (p4 & 1) * 8;
              asm volatile("s_waitcnt lgkmcnt(0)" ::: "memory");
#pragma unroll
              for (int i = 0; i < 4; ++i) y[i] = __builtin_bit_cast(u32x4, ldstr(u0 + i * 1024, u1 + i * 1024));
          }
#pragma unroll
          for (int i = 0; i < 4; ++i) __builtin_amdgcn_raw_buffer_store_b128(y[i], wsr, sto[i], 0, SC1); }
        unsigned ga[4];
        { const int l2 = lane_id(), g4 = l2 >> 4;
#pragma unroll
          for (int ks = 0; ks < 4; ++ks) ga[ks] = (g4 & 1) * (65536 + 32) + (8 * (g4 >> 1) + ((l2 >> 2) & 3)) * 192 + KH * 96 + (l2 & 3) * 8 + (ks ^ rot) * 3072;
          asm volatile("" : "+v"(ga[0]), "+v"(ga[1]), "+v"(ga[2]), "+v"(ga[3])); }
        cs[0] = 0.f; cs[1] = 0.f;
        f32x16 ac2[3] = {};
        SBAR();
#define RW2(f, b, c, k) fw[f][b] = ldstr(lds + ((c) & 3) * 16384 + ga[k] + b * 32, lds + ((c) & 3) * 16384 + ga[k] + b * 32 + 768)
#define M2(f, b, c, k) ac2[b] = MFMA32(fw[f][b], xf[4 * ((c) - 10) + (k)], ac2[b]); SBAR()
#define A2I(c, n) do { constexpr int p_ = c > 10 ? c - 1 : 10; \
        if constexpr (c > 10) { M2(0, 2, p_, 2); } \
        RW2(0, 0, c, 0); SBAR(); RW2(0, 1, c, 0); SBAR(); RW2(0, 2, c, 0); J(c, 0); \
        if constexpr (c > 10) { M2(1, 0, p_, 3); } \
        RW2(1, 0, c, 1); PC(n, 0); J(c, 1); \
        if constexpr (c > 10) { M2(1, 1, p_, 3); } \
        RW2(1, 1, c, 1); PC(n, 1); J(c, 2); \
        if constexpr (c > 10) { M2(1, 2, p_, 3); } \
        RW2(1, 2, c, 1); PC(n, 2); J(c, 3); \
        M2(0, 0, c, 0); RW2(0, 0, c, 2); J(c, 4); M2(0, 1, c, 0); RW2(0, 1, c, 2); J(c, 5); M2(0, 2, c, 0); RW2(0, 2, c, 2); J(c, 6); \
        M2(1, 0, c, 1); RW2(1, 0, c, 3); J(c, 7); M2(1, 1, c, 1); RW2(1, 1, c, 3); J(c, 8); M2(1, 2, c, 1); RW2(1, 2, c, 3); J(c, 9); \
        M2(0, 0, c, 2); J(c, 10); M2(0, 1, c, 2); J(c, 11); } while (0)
        A2I(10, 20); wait_bar<10>();
        A2I(11, 14); wait_bar<10>();
        A2I(12, 15); wait_bar<KH ? 6 : 7>();
        A2I(13, 16); wait_bar<KH ? 6 : 11>();
        A2I(14, 17); wait_bar<KH ? 6 : 11>();
        A2I(15, 18); wait_bar<KH ? 10 : 14>();
        A2I(16, 19); wait_bar<KH ? 10 : 11>();
        A2I(17, 20); wait_bar<KH ? 0 : 4>();
        A2I(18, 20); lds_bar();
        A2I(19, 20);
        M2(0, 2, 19, 2); J(19, 12); M2(1, 0, 19, 3); J(19, 13); M2(1, 1, 19, 3); J(19, 14); M2(1, 2, 19, 3);
        ac2[0] = MFMA8(cf[0], ((u32x2){nmu, nmu}), ac2[0]); ac2[1] = MFMA8(cf[1], ((u32x2){nmu, nmu}), ac2[1]); ac2[2] = MFMA8(cf[2], ((u32x2){nmu, nmu}), ac2[2]); SBAR();
        attn_scores0<0>(at);
        __builtin_amdgcn_s_setprio(KH ? 3 : 2);
        { const float rr2 = rstd * rstd;
#pragma unroll
          for (int b = 0; b < 3; ++b) {
              u32x4 v;
#pragma unroll
              for (int e = 0; e < 4; ++e) v[e] = pk2(ac2[b][2 * e] * rr2 * ac2[b][8 + 2 * e], ac2[b][2 * e + 1] * rr2 * ac2[b][9 + 2 * e]);
              __builtin_amdgcn_raw_buffer_store_b128(v, wsr, t2 + b * 512, 0, SC1);
          } }
    }
    {
        LANE_IDS;
        if constexpr (KH) {
            om = WS_HP + (16 * mq + 4 * qq + (r32 >> 4)) * (272 * 256) + (r32 & 15) * 16 + (192 + 2 * g + hh) * 256;
            orm = WS_HP + (16 * mq + 4 * qq + (lane >> 4)) * (272 * 256) + 256 * 256 + g * 128 + (lane & 15) * 8;
            PIN(om); PIN(orm);
        }
        attn_main<KH>(at, pre, sx, lane * 16, KH ? lane * 16 + (16 - 3 * qq) * 1024 : WS_FH + (rb >> 1) * 128 + (lane & 15) * 8);
    }
    LANE_IDS;
    f32x4 acc[4][5] = {};
    auto mma = [&](const u32x4 (&a)[4], const u32x4 (&b)[5]) {
#pragma unroll
        for (int i = 3; i >= 0; --i)
#pragma unroll
            for (int n = 0; n < 5; ++n) acc[i][n] = MFMA16(__builtin_bit_cast(bf16x8, b[n]), __builtin_bit_cast(bf16x8, a[i]), acc[i][n]);
    };
    if constexpr (!KH) {
        auto hand_over = [&]() {
            LAS char* snd = lds + L_MRG + w * 6656;
#pragma unroll
            for (int i = 0; i < 6; ++i) { const int o = 16 * (i / 3) + 4 * (i % 3); *(LAS f32x4*)(snd + i * 1024 + lane * 16) = (f32x4){at.oo[o], at.oo[o + 1], at.oo[o + 2], at.oo[o + 3]}; }
            *(LAS u32x2*)(snd + 6144 + lane * 8) = (u32x2){__float_as_uint(c[0]), __float_as_uint(c[1])};
        };
        hand_over();
        if (__builtin_expect(attn_overflowed(at), 0)) { attn_redo(at, c, wsr, kvoff, odd, NT0); hand_over(); }
        asm volatile("s_waitcnt lgkmcnt(0)\n\ts_mov_b64 exec, 1\n\tds_add_u32 %0, %1\n\ts_mov_b64 exec, -1" :: "v"(lds0 + L_FLAG + 16 + 4 * w), "v"(1u) : "memory");
        asm volatile("s_waitcnt vmcnt(0)" : "+{v[0:31]}"(pre.a), "+{v[32:63]}"(pre.b), "+{v[212:243]}"(pre.c), "+{v[244:247]}"(pre.d) :: "memory");
        auto kst = [&](int i) { const int k = krot + i; return 12 * w + (k >= 12 ? k - 12 : k); };
        auto loada = [&](int s, u32x4 (&a)[4]) {
#pragma unroll
            for (int i = 0; i < 4; ++i) a[i] = __builtin_amdgcn_raw_buffer_load_b128(wsr, lane * 16, aob + s * 1024 + ((w + i) & 3) * (272 * 256), SC1);
        };
        auto loadb = [&](int s, u32x4 (&b)[5]) {
#pragma unroll
            for (int i = 0; i < 5; ++i) b[i] = __builtin_amdgcn_raw_buffer_load_b128(wsr, lane * 16, bob + s * 1024 + i * (68 * 1024), SC1);
        };
        auto ldsb = [&](int s, u32x4 (&b)[5]) {
#pragma unroll
            for (int n = 0; n < 5; ++n) b[n] = *(const LAS u32x4*)(lds + l_bst(w) + (n * 3 + s) * 1024 + lane * 16);
        };
        u32x4 a0[4] = {pre_frag<0>(pre), pre_frag<1>(pre), pre_frag<2>(pre), pre_frag<3>(pre)}, a1[4] = {pre_frag<9>(pre), pre_frag<10>(pre), pre_frag<11>(pre), pre_frag<12>(pre)};
        u32x4 a2[4] = {pre_frag<18>(pre), pre_frag<19>(pre)}, a3[4], b3[5], a4[4];
        u32x4 b0[5] = {pre_frag<4>(pre), pre_frag<5>(pre), pre_frag<6>(pre), pre_frag<7>(pre), pre_frag<8>(pre)};
        u32x4 b1[5] = {pre_frag<13>(pre), pre_frag<14>(pre), pre_frag<15>(pre), pre_frag<16>(pre), pre_frag<17>(pre)};
        u32x4 b2[5] = {pre_frag<20>(pre), pre_frag<21>(pre), pre_frag<22>(pre), pre_frag<23>(pre), pre_frag<24>(pre)};
        a2[2] = __builtin_amdgcn_raw_buffer_load_b128(wsr, lane * 16, aob + kst(2) * 1024 + ((w + 2) & 3) * (272 * 256), SC1);
        a2[3] = __builtin_amdgcn_raw_buffer_load_b128(wsr, lane * 16, aob + kst(2) * 1024 + ((w + 3) & 3) * (272 * 256), SC1);
        loada(kst(3), a3); loadb(kst(3), b3); SBAR();
        __builtin_amdgcn_s_setprio(0);
        mma(a0, b0); SBAR(); loada(kst(4), a0); loadb(kst(4), b0); SBAR();
        mma(a1, b1); SBAR(); loada(kst(5), a1); loadb(kst(5), b1); SBAR();
        mma(a2, b2); SBAR(); loada(kst(6), a2); loadb(kst(6), b2); SBAR();
        mma(a3, b3); SBAR(); loada(kst(7), a3); loadb(kst(7), b3); SBAR();
        mma(a0, b0); SBAR(); loada(kst(8), a0); loadb(kst(8), b0); SBAR();
        mma(a1, b1); SBAR(); loada(kst(9), a1); loada(kst(10), a4); SBAR();
        mma(a2, b2); SBAR(); loada(kst(11), a2); ldsb(0, b2); SBAR();
        const int xoff = (64 * rb + 16 * w + (lane & 15)) * 1280 + (80 * ct + 4 * (lane >> 4)) * 2;
        u32x2 xv[5];
        mma(a3, b3); SBAR(); ldsb(1, b3);
#pragma unroll
        for (int n = 0; n < 5; ++n) xv[n] = *(const GAS u32x2*)(x + xoff + n * 32);
        SBAR();
        mma(a0, b0); SBAR(); ldsb(2, b0); SBAR();
        asm volatile("s_waitcnt lgkmcnt(0)\n\ts_mov_b64 exec, 1\n\tds_add_u32 %0, %1\n\tds_add_u32 %0, %1 offset:20\n\ts_mov_b64 exec, -1" :: "v"(lds0 + L_FLAG + 12), "v"(1u) : "memory");
        SBAR();
        mma(a1, b2); SBAR(); mma(a4, b3); SBAR(); mma(a2, b0); SBAR();
        lds_wait(lflag + 8, 4u);
#pragma unroll
        for (int i = 3; i >= 1; --i)
#pragma unroll
            for (int n = 0; n < 5; ++n) ((LAS f32x4*)(lds + ((w + i) & 3) * 15360))[(i - 1) * 320 + n * 64 + lane] = acc[i][n];
        asm volatile("s_waitcnt lgkmcnt(0)\n\ts_mov_b64 exec, 1\n\tds_add_u32 %0, %1\n\ts_mov_b64 exec, -1" :: "v"(lds0 + L_FLAG + 32), "v"(1u) : "memory");
        lds_wait(lflag + 8, 8u);
#pragma unroll
        for (int n = 0; n < 5; ++n) {
            LAS f32x4* p = (LAS f32x4*)(lds + w * 15360) + n * 64 + lane;
            const f32x4 xf = {__uint_as_float(xv[n][0] << 16), __uint_as_float(xv[n][0] & 0xffff0000u), __uint_as_float(xv[n][1] << 16), __uint_as_float(xv[n][1] & 0xffff0000u)};
            p[0] = (acc[0][n] + xf) + p[0] + (p[320] + p[640]);
        }
        lds_bar();
    } else {
        auto merge = [&](const f32x4 (&r)[6], float co0, float co1) {
            u32x2 rem[2];
            float wa[2], wb[2];
#pragma unroll
            for (int t = 0; t < 2; ++t) {
                const float co = t ? co1 : co0, cx = fmaxf(c[t], co);
                wa[t] = __builtin_amdgcn_exp2f(c[t] - cx); wb[t] = __builtin_amdgcn_exp2f(co - cx);
                const float inv = __builtin_amdgcn_rcpf(swap_add(hh ? at.oo[16 * t + 8] * wa[t] + r[3 * t + 2][0] * wb[t] : 0.f));
                wa[t] *= inv; wb[t] *= inv;
            }
#pragma unroll
            for (int t = 0; t < 2; ++t) {
                float f[12];
#pragma unroll
                for (int i = 0; i < 12; ++i) f[i] = fma1(at.oo[16 * t + i], wa[t], mul1(r[3 * t + (i >> 2)][i & 3], wb[t]));
                __builtin_amdgcn_raw_buffer_store_b128((u32x4){pk2(f[0], f[1]), pk2(f[2], f[3]), pk2(f[4], f[5]), pk2(f[6], f[7])}, wsr, om + t * (2 * 272 * 256), 0, SC1);
                rem[t] = (u32x2){pk2(f[8], f[9]), pk2(f[10], f[11])};
            }
            const u32x2 rv = {__builtin_amdgcn_permlane32_swap(rem[0][0], rem[1][0], false, false)[0], __builtin_amdgcn_permlane32_swap(rem[0][1], rem[1][1], false, false)[0]};
            __builtin_amdgcn_raw_buffer_store_b64(rv, wsr, orm, 0, SC1);
        };
        { f32x4 r[6];
#pragma unroll
          for (int i = 0; i < 4; ++i) r[i] = (f32x4){at.sa[4 * i], at.sa[4 * i + 1], at.sa[4 * i + 2], at.sa[4 * i + 3]};
#pragma unroll
          for (int i = 4; i < 6; ++i) r[i] = __builtin_bit_cast(f32x4, (u32x4){at.kv[4 * i], at.kv[4 * i + 1], at.kv[4 * i + 2], at.kv[4 * i + 3]});
          merge(r, __uint_as_float(at.kv[24]), __uint_as_float(at.kv[25])); }
        SBAR();
        if (__builtin_expect(attn_overflowed(at), 0)) {
            attn_redo(at, c, wsr, kvoff, odd, 64 - NT0);
            const LAS char* rcv = lds + L_MRG + qq * 6656;
            const u32x2 cp = *(const LAS u32x2*)(rcv + 6144 + lane * 8);
            f32x4 r[6];
#pragma unroll
            for (int i = 0; i < 6; ++i) r[i] = *(const LAS f32x4*)(rcv + i * 1024 + lane * 16);
            merge(r, __uint_as_float(cp[0]), __uint_as_float(cp[1]));
        }
        asm volatile("s_waitcnt lgkmcnt(0)\n\ts_mov_b64 exec, 1\n\tds_add_u32 %0, %1\n\ts_mov_b64 exec, -1\n\ts_waitcnt vmcnt(0)" :: "v"(lds0 + L_FLAG + 12), "v"(1u) : "memory");
        flag_store_if0(at.srd, WS_FO + ((4 * mq + qq) * 4 + (g >> 3)) * 128 + (g & 7) * 8, tag, 0);
        u64 pend[2];
        flag_poll2(at.srd, WS_FO + (rb * 4 + qq) * 128 + (lane & 7) * 8, tag, pend);
        u32x4 a0[4], a1[4], a2[4], a3[4], a4[4];
        auto loado = [&](int s, u32x4 (&a)[4]) {
#pragma unroll
            for (int i = 3; i >= 0; --i) a[i] = __builtin_amdgcn_raw_buffer_load_b128(wsr, lane * 16, aob + (48 + 4 * qq + s) * 1024 + ((qq + i) & 3) * (272 * 256), SC1);
        };
        auto loadr = [&](int i) {
            const unsigned vo = (lane >> 4) * 256 + (lane & 15) * 8, so = aob + (64 + qq) * 1024 + ((qq + i) & 3) * (272 * 256);
            const u32x2 lo = __builtin_amdgcn_raw_buffer_load_b64(wsr, vo, so, SC1), hi = __builtin_amdgcn_raw_buffer_load_b64(wsr, vo + 128, so, SC1);
            a4[i] = (u32x4){lo[0], lo[1], hi[0], hi[1]};
        };
        loado(0, a0); loado(1, a1); loado(2, a2); loado(3, a3); loadr(3); loadr(2); SBAR();
        asm volatile("" : "+v"(a0[3]) : "{v[250:251]}"(pend[0]), "{v[252:253]}"(pend[1]));
#define WOF(s) {pre_frag<5 * s>(pre), pre_frag<5 * s + 1>(pre), pre_frag<5 * s + 2>(pre), pre_frag<5 * s + 3>(pre), pre_frag<5 * s + 4>(pre)}
        { const u32x4 b[5] = WOF(0); mma(a0, b); } SBAR();
        loadr(1); loadr(0); SBAR();
        { const u32x4 b[5] = WOF(1); mma(a1, b); }
        { const u32x4 b[5] = WOF(2); mma(a2, b); }
        lds_wait(lflag + 3, 8u); SBAR();
        { const u32x4 b[5] = WOF(3); mma(a3, b); }
        LAS f32x4* red = (LAS f32x4*)(lds + 65536) + lane;
        { const u32x4 b[5] = WOF(4);
#pragma unroll
          for (int i = 3; i >= 0; --i) {
#pragma unroll
              for (int n = 0; n < 5; ++n) {
                  acc[i][n] = MFMA16(__builtin_bit_cast(bf16x8, b[n]), __builtin_bit_cast(bf16x8, a4[i]), acc[i][n]);
                  if (i < 3) red[((qq + i + 1) & 3) * 960 + i * 320 + n * 64] = acc[i + 1][n];
                  SBAR();
              }
          } }
        lds_bar();
        const int xoff = (64 * rb + 16 * qq + (lane & 15)) * 1280 + (80 * ct + 4 * (lane >> 4)) * 2;
        unsigned o[5][2];
#pragma unroll
        for (int n = 0; n < 5; ++n) {
            const LAS f32x4* p = red + qq * 960 + n * 64;
            const f32x4 sum = (acc[0][n] + p[0]) + (p[320] + p[640]) + ((const LAS f32x4*)(lds + qq * 15360))[n * 64 + lane];
            o[n][0] = pk2(sum[0], sum[1]); o[n][1] = pk2(sum[2], sum[3]);
        }
#pragma unroll
        for (int n = 0; n < 4; n += 2) {
            const auto lo = __builtin_amdgcn_permlane16_swap(o[n][0], o[n + 1][0], false, false), hi = __builtin_amdgcn_permlane16_swap(o[n][1], o[n + 1][1], false, false);
            *(GAS u32x4*)(out + xoff + n * 32 + ((lane >> 4) & 1) * 24) = (u32x4){lo[0], hi[0], lo[1], hi[1]};
        }
        *(GAS u32x2*)(out + xoff + 128) = (u32x2){o[4][0], o[4][1]};
    }
}

__global__ __launch_bounds__(512, 2) void fused_block(const char* x, const char* wqkv, const char* wo, const char* wg, const char* wu, const char* wd,
                                                      char* out, char* ws) {
    __shared__ __attribute__((aligned(16))) char smem[L_TOTAL];
    const int w = __builtin_amdgcn_readfirstlane(threadIdx.x >> 6);
    {
      const int lane = threadIdx.x & 63, bid = blockIdx.x, rr = bid >> 3;
      u64 pc0; asm volatile("s_getpc_b64 %0" : "=s"(pc0));
      glds4(min((int)(threadIdx.x & ~31u) + rr, CODE_LINES - 1) * 128, (const char*)pc0, (unsigned)(size_t)(LAS char*)smem + L_TR);
      SBAR();
      const int mi = rr & 7, ji = rr >> 3, m = 8 * (bid & 1) + mi, j = 4 * ((bid & 7) >> 1) + ji, qq = w & 3, kh = w >> 2;
      const int s0 = first_chunk(mi, ji);
      Fetch1 f1; fetch1_lanes(f1, kh, lane);
      const unsigned base = fetch1_off(kh, qq, m, j), cs = kh ? 128 : 64 * 3840;
      unsigned so0 = base + s0 * cs, so1 = base + rot10(s0, 1) * cs, d = (unsigned)(size_t)(LAS char*)smem + (1 - kh) * 65536 + qq * 4096;
      asm volatile("" : "+v"(f1.vo[0]), "+v"(f1.vo[1]), "+v"(f1.vo[2]), "+v"(f1.vo[3]), "+s"(so0), "+s"(so1), "+s"(d));
      f1.srd = make_srd(kh ? x : wqkv, kh ? 2048 * 1280 : 640 * 3840);
      dma16<0>(f1.vo[0], f1.srd, so0, d); dma16<1>(f1.vo[1], f1.srd, so0, 0); dma16<2>(f1.vo[2], f1.srd, so0, 0); dma16<3>(f1.vo[3], f1.srd, so0, 0);
      {
        const char* pb = w == 0 ? wg : (w == 1 ? wu : (w == 2 ? wd : (w == 3 ? wo : (w == 4 ? out : ws + (w - 5) * (WS_END / 3 / 8192 * 8192)))));
        const unsigned st = w < 3 ? 30720 : (w == 3 ? 12800 : (w == 4 ? 40960 : WS_END / 3 / 64 / 128 * 128));
        glds4(((lane & 32) + rr) * st, pb, (unsigned)(size_t)(LAS char*)smem + L_TR + 256); }
      __builtin_amdgcn_s_barrier();
      dma16<0>(f1.vo[0], f1.srd, so1, d + 16384);
      dma16<1>(f1.vo[1], f1.srd, so1, 0);
      asm volatile("" : "+s"(x), "+s"(wqkv), "+s"(wo), "+s"(wg), "+s"(wu), "+s"(wd), "+s"(out), "+s"(ws));
    }
    if (w >= 4) program<1>(x, wqkv, wo, wg, wu, wd, out, ws, (LAS char*)smem, w);
    else program<0>(x, wqkv, wo, wg, wu, wd, out, ws, (LAS char*)smem, w);
}

extern "C" void kernel_launch(void* const* d_in, const int* in_sizes, int n_in, void* d_out, int out_size, void* d_ws, size_t ws_size,
                              hipStream_t stream) {
    fused_block<<<256, 512, 0, stream>>>((const char*)d_in[0], (const char*)d_in[1], (const char*)d_in[2], (const char*)d_in[4],
                                         (const char*)d_in[5], (const char*)d_in[6], (char*)d_out, (char*)d_ws);
}
```
